```python
import torch

def _softmax(x, axis=-1):
    return torch.softmax(x, dim=axis)

def _gelu(x):
    return torch.nn.functional.gelu(x, approximate='tanh')

def _silu(x):
    return torch.nn.functional.silu(x)

def _sigmoid(x):
    return torch.sigmoid(x)

def _layernorm(x, axis=-1, eps=1e-05):
    m = x.mean(dim=axis, keepdim=True)
    v = x.var(dim=axis, keepdim=True, unbiased=False)
    return (x - m) / torch.sqrt(v + eps)

def _scatter_add(tgt, idx, src, axis=0):
    return tgt.clone().index_add_(axis, idx, src)

def _segment_sum(src, seg, n):
    out = torch.zeros((n,) + src.shape[1:], dtype=src.dtype)
    return out.index_add_(0, seg, src)

def _pad(x, pw):
    flat = tuple((v for pair in reversed(pw) for v in pair))
    return torch.nn.functional.pad(x, flat)
kernel_name = 'concat_heads__memory_aug_recurrent_mem_tr__mha_block__prompt'

def setup_inputs():

    _w = lambda a: (lambda t: t.to(torch.bfloat16) if t.is_floating_point() else t)(torch.as_tensor(a))

    def _A_setup():
        B, S, Dm = (1, 2048, 1024)
        H, Dh = (32, 128)
        M, seg = (8, 32)
        seg = min(seg, S)
        S = S // seg * seg
        return {'x': _w((lambda *s: torch.randn(*s, dtype=torch.bfloat16) * 0.05)(B, S, Dm)), 'mem0': _w((lambda *s: torch.randn(*s, dtype=torch.bfloat16) * 1.6)(B, M, Dm)), 'Wq': _w((lambda *s: torch.randn(*s, dtype=torch.bfloat16) * 0.8)(Dm, H, Dh) * Dm ** (-0.5)), 'Wk': _w((lambda *s: torch.randn(*s, dtype=torch.bfloat16) * 0.8)(Dm, H, Dh) * Dm ** (-0.5)), 'Wv': _w((lambda *s: torch.randn(*s, dtype=torch.bfloat16) * 1.6)(Dm, H, Dh) * Dm ** (-0.5)), 'Wo': _w((lambda *s: torch.randn(*s, dtype=torch.bfloat16) * 1.6)(H, Dh, Dm) * (H * Dh) ** (-0.5)), 'seg_len': seg}
    _A_in = _A_setup()

    def _B_setup():
        B, S, D, H = (1, 2048, 1024, 32)
        return {'x': _w((lambda *s: torch.randn(*s, dtype=torch.bfloat16) * 0.2)(B, S, D)), 'wqkv': _w((lambda *s: torch.randn(*s, dtype=torch.bfloat16) * 0.2)(D, 3 * D)), 'wo': _w((lambda *s: torch.randn(*s, dtype=torch.bfloat16) * 0.05)(D, D))}
    _B_in = _B_setup()
    return {'A_x': _A_in['x'], 'A_mem0': _A_in['mem0'], 'A_Wq': _A_in['Wq'], 'A_Wk': _A_in['Wk'], 'A_Wv': _A_in['Wv'], 'A_Wo': _A_in['Wo'], 'A_seg_len': _A_in['seg_len'], 'B_x': _B_in['x'], 'B_wqkv': _B_in['wqkv'], 'B_wo': _B_in['wo']}

def reference(A_x, A_mem0, A_Wq, A_Wk, A_Wv, A_Wo, A_seg_len, B_x, B_wqkv, B_wo):

    def _A_ref(x, mem0, Wq, Wk, Wv, Wo, seg_len):
        B, S, Dm = x.shape
        H, Dh = (Wq.shape[1], Wq.shape[2])
        M = mem0.shape[1]
        scale = Dh ** (-0.5)
        n_seg = S // seg_len
        L = M + seg_len
        idx = torch.arange(L)
        mask = (idx[:, None] < M) | (idx[:, None] >= idx[None, :])
        mem = mem0
        outs = []
        for s in range(n_seg):
            xs = x[:, s * seg_len:(s + 1) * seg_len, :].clone()
            h = torch.concatenate([mem, xs], axis=1)
            q = torch.einsum('btd,dhe->bhte', h, Wq)
            k = torch.einsum('btd,dhe->bhte', h, Wk)
            v = torch.einsum('btd,dhe->bhte', h, Wv)
            scores = torch.einsum('bhqe,bhke->bhqk', q, k) * scale
            scores = torch.where(mask[None, None], scores, -1000000000.0)
            p = _softmax(scores, axis=-1)
            o = torch.einsum('bhqk,bhke->bhqe', p, v)
            o = torch.einsum('bhte,hed->btd', o, Wo)
            mem = o[:, :M, :].clone()
            outs.append(o[:, M:, :])
        return torch.concatenate(outs, axis=1)

    def _B_ref(x, wqkv, wo):
        B, S, D = x.shape
        H = 32
        Dh = D // H
        qkv = (lambda x, *d: x.permute(*d))((x @ wqkv).reshape(B, S, 3, H, Dh), 2, 0, 3, 1, 4)
        q, k, v = (qkv[0], qkv[1], qkv[2])
        scores = torch.einsum('bhqd,bhkd->bhqk', q, k) * Dh ** (-0.5)
        p = _softmax(scores, axis=-1)
        o = torch.einsum('bhqk,bhkd->bhqd', p, v)
        return (lambda x, *d: x.permute(*d))(o, 0, 2, 1, 3).reshape(B, S, D) @ wo
    a = _A_ref(A_x, A_mem0, A_Wq, A_Wk, A_Wv, A_Wo, A_seg_len)
    b = _B_ref(B_x, B_wqkv, B_wo)
    return torch.concatenate([a, b], axis=-1)
```

```cpp
#include <hip/hip_runtime.h>
#include <hip/hip_bf16.h>
#include <stdint.h>
#include <stdio.h>

#define LAS __attribute__((address_space(3)))
#define GAS __attribute__((address_space(1)))
typedef unsigned v4u __attribute__((ext_vector_type(4)));
typedef unsigned v2u __attribute__((ext_vector_type(2)));
typedef unsigned v8u __attribute__((ext_vector_type(8)));
typedef float f32x4 __attribute__((ext_vector_type(4)));
typedef float f32x2 __attribute__((ext_vector_type(2)));
typedef float f32x16 __attribute__((ext_vector_type(16)));
typedef __bf16 bf16x8 __attribute__((ext_vector_type(8)));
typedef __bf16 bf16x16 __attribute__((ext_vector_type(16)));
typedef __bf16 bf16x2 __attribute__((ext_vector_type(2)));
typedef short v4i16 __attribute__((ext_vector_type(4)));
typedef unsigned short u16;
typedef GAS unsigned gu32;
#define RLX_AGENT __ATOMIC_RELAXED, __HIP_MEMORY_SCOPE_AGENT

#define DBG_STAMP 0

namespace {

constexpr int SEQ = 2048, DM = 1024, NH = 32, DH = 128, NSEG = 64;
constexpr int LDX = 3 * NH * DH;
constexpr size_t COMM_BYTES = 6u << 20;
constexpr size_t CTL_OFF = COMM_BYTES;
constexpr size_t XQKV_OFF = CTL_OFF + (2u << 20);
constexpr size_t OX_OFF = XQKV_OFF + (size_t)SEQ * LDX * 2;
constexpr size_t BQKV_OFF = OX_OFF + (size_t)SEQ * 4096 * 2;
constexpr size_t BO_OFF = BQKV_OFF + (size_t)SEQ * 3072 * 2;
constexpr size_t SLAB_OFF = BO_OFF + (size_t)SEQ * 1024 * 2 + 4096;
constexpr size_t WS_END = SLAB_OFF + (size_t)8 * SEQ * 1024 * 2;
constexpr int CTL_POL = 0, CTL_ABORT = 64, CTL_RB = 128  , CTL_B = 640, CTL_ATT = 704  , CTL_AQ = 1216  , CTL_GQ = 1472, CTL_WORDS = 1536;
constexpr int CTL_STAMP = 16384;
constexpr unsigned SENT = 0xFFFFFFFFu;
constexpr int AUX_SC1 = 16;
constexpr unsigned SPIN_CAP = 1u << 15;
constexpr int MEM_SLOT = 2 * 65536, QKV_BASE = 2 * 262144, QKV_SLOT = 2 * 196608;

__device__ __forceinline__ int vo(int o) { return ((o >> 13) << 14) | (o & 8191); }
__device__ __forceinline__ unsigned cvt_pk(float lo, float hi) {
  f32x2 v = {lo, hi};
  return __builtin_bit_cast(unsigned, __builtin_convertvector(v, bf16x2));
}
__device__ __forceinline__ f32x4 mfma16(v4u a, v4u b, f32x4 c) {
  return __builtin_amdgcn_mfma_f32_16x16x32_bf16(__builtin_bit_cast(bf16x8, a), __builtin_bit_cast(bf16x8, b), c, 0, 0, 0);
}
__device__ __forceinline__ f32x4 smfmac(v4u a, v8u b, f32x4 c, int idx) {
  return __builtin_amdgcn_smfmac_f32_16x16x64_bf16(__builtin_bit_cast(bf16x8, a), __builtin_bit_cast(bf16x16, b), c, idx, 0, 0);
}
#define MF_Z(d, a, b, BC) asm volatile("v_mfma_f32_16x16x32_bf16 %0, %1, %2, 0" : "=&v"(d) : "v"(a), BC(b))
#define MF_C(d, a, b, c) asm volatile("v_mfma_f32_16x16x32_bf16 %0, %1, %2, %3" : "=&v"(d) : "v"(a), "v"(b), "v"(c))
#define MF_A(d, a, b, BC) asm volatile("v_mfma_f32_16x16x32_bf16 %0, %1, %2, %0" : "+v"(d) : "v"(a), BC(b))
#define SMF_A(d, a, b, i) asm volatile("v_smfmac_f32_16x16x64_bf16 %0, %1, %2, %3" : "+v"(d) : "v"(a), "a"(b), "v"(i))
#define NOPS(n) asm volatile("s_nop %0" ::"i"((n)-1))
#define PIN1(a) asm volatile("" : "+v"(a))
#define PIN2(a, b) asm volatile("" : "+v"(a), "+v"(b))
__device__ __forceinline__ v2u trread(LAS unsigned char* p) {
  return __builtin_bit_cast(v2u, __builtin_amdgcn_ds_read_tr16_b64_v4i16((LAS v4i16*)p));
}
__device__ __forceinline__ unsigned umax3(unsigned a, unsigned b, unsigned c) { return max(max(a, b), c); }
__device__ __forceinline__ unsigned umax4(unsigned m, v4u v) { return umax3(umax3(m, v.x, v.y), v.z, v.w); }
__device__ __forceinline__ float fbits(unsigned u) { return __builtin_bit_cast(float, u); }
__device__ __forceinline__ unsigned ubits(float f) { return __builtin_bit_cast(unsigned, f); }
#define SB() __builtin_amdgcn_sched_barrier(0)
#define BAR() asm volatile("s_waitcnt lgkmcnt(0)\n\ts_barrier" ::: "memory")
__device__ __forceinline__ unsigned clk32() {
  unsigned long long t;
  asm volatile("s_memtime %0\n\ts_waitcnt lgkmcnt(0)" : "=s"(t)::"memory");
  return (unsigned)t;
}

__global__ void __launch_bounds__(256) k_init(unsigned char* base, const u16* mem0) {
  __amdgpu_buffer_rsrc_t rs = __builtin_amdgcn_make_buffer_rsrc(base, 0, (int)(COMM_BYTES + 65536), 0x00020000);
  const v4u s = {SENT, SENT, SENT, SENT};
  const int per = (int)(COMM_BYTES / 256);
  for (int o = blockIdx.x * per + threadIdx.x * 16; o < (int)(blockIdx.x + 1) * per; o += 4096) {
    v4u v = s;
    const int u = o - 3 * MEM_SLOT;
    if (u >= 0 && u < 32768) {
      const int lo = ((u >> 14) << 13) | (u & 8191);
      const int r = lo >> 7, rp = (lo >> 4) & 7;
      v = *(const GAS v4u*)(mem0 + (4 * (r & 1) + 2 * (rp >> 2) + (rp & 1)) * DM + 16 * (r >> 1) + 8 * ((rp >> 1) & 1));
      v = v4u{min(v.x, SENT - 1u), min(v.y, SENT - 1u), min(v.z, SENT - 1u), min(v.w, SENT - 1u)};
    }
    __builtin_amdgcn_raw_buffer_store_b128(v, rs, o, 0, AUX_SC1);
  }
  if (blockIdx.x == 255 && threadIdx.x < CTL_WORDS / 64)
    __hip_atomic_store((gu32*)(base + CTL_OFF) + threadIdx.x * 64, threadIdx.x == 0 ? 2u : 0u, RLX_AGENT);
}

struct ChainArgs {
  const u16* Wq; const u16* Wk; const u16* Wv; const u16* Wo;
  unsigned char* base;
};
constexpr int L_RED = 0;
constexpr int L_SCHED = 13824;
constexpr int L_QF = 14336;
constexpr int L_KVF = L_QF + 8192;
constexpr int L_KX = L_KVF + 4096;
constexpr int L_VX = L_KX + 16384;
constexpr int L_END = L_VX + 16384;
constexpr int L_WST = 0;
constexpr int CHAIN_LDS = 96 * 1024;

__device__ __forceinline__ bool spin_fail(unsigned& spins, gu32* ctl, bool& dead) {
  if (dead) return true;
  ++spins;
  if (spins > SPIN_CAP) { __hip_atomic_store(ctl + CTL_ABORT, 1u, RLX_AGENT); dead = true; return true; }
  if ((spins & 63u) == 0u && __builtin_amdgcn_readfirstlane(__hip_atomic_load(ctl + CTL_ABORT, RLX_AGENT))) { dead = true; return true; }
  return false;
}

#define LATE 20
#define RELOCK 150
#define EPS1 12
#define EPS2 6
#define EPS3 16
#define SLIP 8
#define OXAUX 0
#define SAFE 4
#define PFLAG 4
#define D1_0 46
#define D2_0 88
#define D3_0 100

#define STAMP(k)
#define STAMPX(k) do { if (s >= 32 && s < 40) { SB(); unsigned c_ = clk32(); SB(); if (lane == 0) stamp[((s - 32) * 4 + w) * 16 + (k)] = c_; } } while (0)
#define STAMP2(k)
#define STAMP3(k)
#define STAMP4(k)

template <int W> __device__ __forceinline__ void chain_loop(const ChainArgs& a, LAS unsigned char* L, int cid, const v8u (&wqkv)[4][6], const v4u (&wo)[4][4]);

__device__ __forceinline__ void chain_role(const ChainArgs& a, LAS unsigned char* L, int cid) {
  const int tid = threadIdx.x, lane = tid & 63, w = __builtin_amdgcn_readfirstlane(tid >> 6);
  const int h = (cid & 3) * 8 + (cid >> 4), g = (cid >> 2) & 3;
  const int l15 = lane & 15, lg = lane >> 4;
  if (cid == 0 && tid == 0) {
    unsigned lat[2] = {~0u, ~0u};
    for (int rep = 0; rep < 3; ++rep)
      for (int b = 0; b < 2; ++b) {
        unsigned* p = (unsigned*)(a.base + CTL_OFF + 32768 + b * 8192 + (rep & 1) * 4096 + (rep >> 1) * 2048);
        unsigned v;
        asm volatile("global_store_dword %0, %1, off sc1\n\ts_waitcnt vmcnt(0)" ::"v"(p), "v"(0u) : "memory");
        unsigned t0 = clk32();
        asm volatile("global_load_dword %0, %1, off sc1\n\ts_waitcnt vmcnt(0)" : "=v"(v) : "v"(p) : "memory");
        lat[b] = min(lat[b], clk32() - t0 + v);
      }
    __hip_atomic_store((gu32*)(a.base + CTL_OFF) + CTL_POL, lat[1] < lat[0] ? 0u : 1u, RLX_AGENT);
  }
  v8u wqkv[4][6];
  v4u wo[4][4];
  {
    LAS unsigned char* st = L + L_WST + w * 16384;
    const int q4 = l15 >> 2, p4 = lane & 3;
    v4u ld[2][16];
    auto issue = [&](int m, v4u(&d)[16]) {
      if (m < 3) {
        const u16* W = (m == 0 ? a.Wq : m == 1 ? a.Wk : a.Wv) + (size_t)(256 * w + (lane >> 2)) * 4096 + h * 128 + 32 * g + 8 * (lane & 3);
#pragma unroll
        for (int i = 0; i < 16; ++i) d[i] = *(const GAS v4u*)(W + (size_t)i * 16 * 4096);
      } else {
        const u16* W = a.Wo + (size_t)(h * 128 + (lane >> 3)) * 1024 + 256 * g + 64 * w + 8 * (lane & 7);
#pragma unroll
        for (int i = 0; i < 16; ++i) d[i] = *(const GAS v4u*)(W + (size_t)i * 8 * 1024);
      }
      SB();
    };
    issue(0, ld[0]);
    issue(1, ld[1]);
#pragma unroll
    for (int m = 0; m < 4; ++m) {
      v4u(&d)[16] = ld[m & 1];
      if (m < 3) {
#pragma unroll
        for (int i = 0; i < 16; ++i) {
          v2u ev = {__builtin_amdgcn_perm(d[i].y, d[i].x, 0x05040100u), __builtin_amdgcn_perm(d[i].w, d[i].z, 0x05040100u)};
          v2u od = {__builtin_amdgcn_perm(d[i].y, d[i].x, 0x07060302u), __builtin_amdgcn_perm(d[i].w, d[i].z, 0x07060302u)};
          LAS unsigned char* p = st + (16 * i + (lane >> 2)) * 64 + (lane & 3) * 8;
          *(LAS v2u*)p = ev;
          *(LAS v2u*)(p + 32) = od;
        }
      } else {
#pragma unroll
        for (int i = 0; i < 16; ++i) *(LAS v4u*)(st + i * 1024 + lane * 16) = d[i];
      }
      SB();
      if (m + 2 < 4) issue(m + 2, d);
      asm volatile("s_waitcnt lgkmcnt(0)" ::: "memory");
      if (m < 3) {
        LAS unsigned char* rb = st + (16 * (lg >> 1) + 4 * (lg & 1) + (q4 & 1) + 8 * (q4 >> 1)) * 64 + p4 * 8;
#pragma unroll
        for (int kk = 0; kk < 4; ++kk)
#pragma unroll
          for (int T = 0; T < 2; ++T) {
            v2u r0 = trread(rb + (64 * kk) * 64 + T * 32), r1 = trread(rb + (64 * kk + 2) * 64 + T * 32);
            v2u r2 = trread(rb + (64 * kk + 32) * 64 + T * 32), r3 = trread(rb + (64 * kk + 34) * 64 + T * 32);
            v8u v = {r0.x, r0.y, r1.x, r1.y, r2.x, r2.y, r3.x, r3.y};
            asm volatile("" : "=a"(wqkv[kk][m * 2 + T]) : "0"(v));
          }
      } else {
        LAS unsigned char* rb = st + (8 * lg + q4) * 128 + p4 * 16;
#pragma unroll
        for (int ct = 0; ct < 4; ++ct)
#pragma unroll
          for (int kk = 0; kk < 4; ++kk) {
            v2u r0 = trread(rb + (32 * kk) * 128 + (ct >> 1) * 64 + (ct & 1) * 8), r1 = trread(rb + (32 * kk + 4) * 128 + (ct >> 1) * 64 + (ct & 1) * 8);
            v4u v = {r0.x, r0.y, r1.x, r1.y};
            asm volatile("" : "=a"(wo[ct][kk]) : "0"(v));
          }
      }
      asm volatile("s_waitcnt lgkmcnt(0)" ::: "memory");
    }
  }
  if (w == 0) chain_loop<0>(a, L, cid, wqkv, wo);
  else if (w == 1) chain_loop<1>(a, L, cid, wqkv, wo);
  else if (w == 2) chain_loop<2>(a, L, cid, wqkv, wo);
  else chain_loop<3>(a, L, cid, wqkv, wo);
}

__device__ __forceinline__ float fmx(float a, float b) { return __builtin_elementwise_maximum(a, b); }
__device__ __forceinline__ float fadd(float a, float b) { float r; asm("v_add_f32 %0, %1, %2" : "=v"(r) : "v"(a), "v"(b)); return r; }
#define LD16(rs, vo_, imm, so) __builtin_amdgcn_raw_buffer_load_b128(rs, (vo_) + (imm), so, AUX_SC1)

__device__ __forceinline__ unsigned rtc32() {
  unsigned long long t;
  asm volatile("s_memrealtime %0\n\ts_waitcnt lgkmcnt(0)" : "=s"(t)::"memory");
  return (unsigned)t;
}
__device__ __forceinline__ void wait_sched(unsigned T, unsigned extra) {
  unsigned now = rtc32();
  if ((int)(now - T) > SLIP) T = now + LATE;
  T += extra;
  for (int i = 0; i < 4096 && (int)(now - T) < 0; ++i) now = rtc32();
}
__device__ __forceinline__ unsigned wave_umax(unsigned v) {
  v = max(v, (unsigned)__builtin_amdgcn_update_dpp(0, (int)v, 0x111, 0xf, 0xf, true));
  v = max(v, (unsigned)__builtin_amdgcn_update_dpp(0, (int)v, 0x112, 0xf, 0xf, true));
  v = max(v, (unsigned)__builtin_amdgcn_update_dpp(0, (int)v, 0x114, 0xf, 0xf, true));
  v = max(v, (unsigned)__builtin_amdgcn_update_dpp(0, (int)v, 0x118, 0xf, 0xf, true));
  return max(max(__builtin_amdgcn_readlane(v, 15), __builtin_amdgcn_readlane(v, 31)), max(__builtin_amdgcn_readlane(v, 47), __builtin_amdgcn_readlane(v, 63)));
}
__device__ __forceinline__ int imax(int a, int b) { return a > b ? a : b; }
__device__ __forceinline__ int imin(int a, int b) { return a < b ? a : b; }

template <int W>
__device__ __forceinline__ void chain_loop(const ChainArgs& a, LAS unsigned char* L, int cid, const v8u (&wqkv)[4][6], const v4u (&wo)[4][4]) {
  const int tid = threadIdx.x, lane = tid & 63;
  constexpr int w = W;
  const int h = (cid & 3) * 8 + (cid >> 4), g = (cid >> 2) & 3, cu = h * 4 + g;
  const int l15 = lane & 15, lg = lane >> 4;
  gu32* ctl = (gu32*)(a.base + CTL_OFF);
  bool dead = false;
  unsigned pol;
  for (unsigned spins = 0; (pol = __builtin_amdgcn_readfirstlane(__hip_atomic_load(ctl + CTL_POL, RLX_AGENT))) > 1u;)
    if (spin_fail(spins, ctl, dead)) break;
  pol &= 1u;
  __amdgpu_buffer_rsrc_t rs = __builtin_amdgcn_make_buffer_rsrc(a.base, 0, (int)COMM_BYTES, 0x00020000);
  __amdgpu_buffer_rsrc_t rx = __builtin_amdgcn_make_buffer_rsrc(a.base + XQKV_OFF, 0, SEQ * LDX * 2, 0x00020000);
  __amdgpu_buffer_rsrc_t ro = __builtin_amdgcn_make_buffer_rsrc(a.base + OX_OFF, 0, SEQ * 4096 * 2, 0x00020000);
  const int so_w0 = (int)(pol << 13);
  auto so_part = [&](int slot) { return ((2 + slot) << 20) + (int)(((pol ^ 1u ^ (unsigned)(slot >> 1)) & 1u) << 13); };
  const int s1_half = (l15 >> 1) & 1;
  int a_mem = vo(w * 4096 + lane * 16);
  int sidx = s1_half ? 0xEEEE : 0x4444;
  const int pub_u = (w == 0 ? lg * 128 : 512 + lg * 256 + (w == 2 ? 128 : 0)) + l15 * 8;
  const int a_pub = vo(cu * 1536 + pub_u);
  int a_g2[3], l_g2[3], l_g2p[3];
#pragma unroll
  for (int i = 0; i < 3; ++i) {
    const int o = (w & 1) * 3072 + i * 1024 + lane * 16, kk = o / 1536, u = o % 1536;
    a_g2[i] = vo(h * 6144 + o);
    l_g2[i] = u < 512 ? (L_QF + kk * 1024 + (u >> 7) * 256 + (u & 127)) : (L_KVF + kk * 1024 + u - 512);
    l_g2p[i] = u < 512 ? 4096 : 0;
  }
  const int a_ps = vo((32 * g + 8 * w + 4 * (l15 >> 3) + 2 * (lg >> 1) + ((l15 >> 2) & 1)) * 4096 + h * 128 + (lg & 1) * 64 + (l15 & 3) * 16);
  int a_pg = vo(cu * 4096 + lane * 16);
  const int a_ms = (lane < 32 && l15 < 8) ? vo(cu * 128 + (4 * ((l15 >> 1) & 1) + 2 * (l15 >> 2) + (l15 & 1)) * 16 + lg * 8) : 0x40000000;
  v4u sel;
#pragma unroll
  for (int j = 0; j < 4; ++j) sel[j] = l15 == 2 * j ? 0x00003F80u : l15 == 2 * j + 1 ? 0x3F800000u : 0u;
  PIN1(sel);
  const int a_mr = lane < 8 ? vo(cu * 128 + lane * 16) : 0x40000000;
  const int a_ts = lane == 0 ? vo(16384 + cu * 128) : 0x40000000;
  const int a_tg = vo(16384 + lane * 128);
  const int x_kv = (lg * LDX + (w == 2 ? 4096 : 8192) + h * 128 + 8 * l15) * 2;
  const int x_q = ((8 * g + 4 * (w & 1) + lg) * LDX + h * 128 + 8 * l15) * 2;
  const int l_q = L_QF + l15 * 256 + (8 + 4 * (w & 1) + lg) * 16;
  int a_ox = l15 >= 8 ? ((8 * g + l15 - 8) * 4096 + h * 128 + 32 * w + 4 * lg) * 2 : 0x40000000;
  f32x4 msk[3];
#pragma unroll
  for (int r = 0; r < 4; ++r) {
    const int qi = 8 * g + l15 - 8;
    msk[0][r] = (l15 < 8 || 4 * lg + r <= qi) ? 0.f : -1e30f;
    msk[1][r] = (l15 < 8 || 16 + 4 * lg + r <= qi) ? 0.f : -1e30f;
    msk[2][r] = lg < 2 ? 0.f : -1e30f;
  }
  v4u ones = {0x3F803F80u, 0x3F803F80u, 0x3F803F80u, 0x3F803F80u};
  v4u sentv = {SENT, SENT, SENT, SENT};
  v4u bm = {0u, 0u, 0u, 0u}, bm2 = {0u, 0u, 0u, 0u};
  PIN2(ones, sentv);
  PIN2(bm, bm2);
  PIN2(msk[0], msk[1]);
  PIN2(msk[2], sidx);
  constexpr float CEXP = 0.08838834764831845f * 1.4426950408889634f;
  LAS unsigned* sched = (LAS unsigned*)(L + L_SCHED);

  v4u pf[9];
  auto pf_issue = [&](int s) {
    const int so = s * 32 * LDX * 2;
#pragma unroll
    for (int i = 0; i < 8; ++i) pf[i] = __builtin_amdgcn_raw_buffer_load_b128(rx, x_kv, so + i * 4 * LDX * 2, AUX_SC1);
    pf[8] = __builtin_amdgcn_raw_buffer_load_b128(rx, x_q, so, AUX_SC1);
    SB();
  };
  auto pf_write = [&](int s) {
    LAS unsigned char* d = L + (w == 2 ? L_KX : L_VX) + (s & 1) * 8192 + l15 * 256;
#pragma unroll
    for (int i = 0; i < 8; ++i) {
      const int row = 4 * i + lg;
      *(LAS v4u*)(d + (row >> 4) * 4096 + (((row & 15) ^ (l15 & 7)) * 16)) = pf[i];
    }
    *(LAS v4u*)(L + l_q + (s & 1) * 4096) = pf[8];
  };
  BAR();
  auto rb_wait = [&](unsigned have, int rb) {
    for (unsigned spins = 0; have < 48u; have = __builtin_amdgcn_readfirstlane(__hip_atomic_load(ctl + CTL_RB + 64 * rb, RLX_AGENT))) {
      __builtin_amdgcn_s_sleep(8);
      if (spin_fail(spins, ctl, dead)) break;
    }
    __builtin_amdgcn_fence(__ATOMIC_ACQUIRE, "wavefront");
  };
  if (w >= 2) { rb_wait(0u, 0); pf_issue(0); pf_write(0); }
  const unsigned tb = rtc32() - (1u << 24);
  int c1 = 0, c2 = 0, c3 = 0, p1 = 0, p2 = 0, p3 = 0, pTm = 1 << 30, d1 = D1_0, d2 = D2_0, d3 = D3_0, m1 = D1_0, m2 = D2_0, m3 = D3_0;
  unsigned I1 = tb, I2 = tb, I3 = tb;
  BAR();

#define CNT(k)
  for (int s = 0; s < NSEG; ++s) {
    const int par = s & 1;
    STAMP(0);
    v4u zx[4], kxf[2][4], mf[4], vf[2][4];
    LAS unsigned char* vb0 = L + L_VX + par * 8192 + lg * 256 + ((l15 ^ lg) * 16);
    LAS unsigned char* vb1 = L + L_VX + par * 8192 + 1024 + lg * 256 + ((l15 ^ (4 + lg)) * 16);
    const int so_m = so_w0 + ((s + 3) & 3) * MEM_SLOT;
    wait_sched(I1, 0);
    STAMP4(0);
    PIN1(a_mem);
#pragma unroll
    for (int kk = 0; kk < 4; ++kk) mf[kk] = LD16(rs, a_mem, kk * 1024, so_m);
    SB();
#pragma unroll
    for (int kk = 0; kk < 4; ++kk)
#pragma unroll
      for (int kt = 0; kt < 2; ++kt) vf[kt][kk] = *(LAS v4u*)(((kk & 1) ? vb1 : vb0) + kt * 4096 + (kk >> 1) * 2048);
    SB();
#define ZTILE(ct) \
    MF_Z(zc[(ct) & 1][0], vf[0][0], wo[ct][0], "a"); MF_Z(zc[(ct) & 1][1], vf[1][0], wo[ct][0], "a"); \
    MF_A(zc[(ct) & 1][0], vf[0][1], wo[ct][1], "a"); MF_A(zc[(ct) & 1][1], vf[1][1], wo[ct][1], "a"); \
    MF_A(zc[(ct) & 1][0], vf[0][2], wo[ct][2], "a"); MF_A(zc[(ct) & 1][1], vf[1][2], wo[ct][2], "a"); \
    MF_A(zc[(ct) & 1][0], vf[0][3], wo[ct][3], "a"); MF_A(zc[(ct) & 1][1], vf[1][3], wo[ct][3], "a")
#define ZCVT(ct) \
    PIN2(zc[(ct) & 1][0], zc[(ct) & 1][1]); \
    zx[ct] = v4u{cvt_pk(zc[(ct) & 1][0][0], zc[(ct) & 1][0][1]), cvt_pk(zc[(ct) & 1][0][2], zc[(ct) & 1][0][3]), \
                 cvt_pk(zc[(ct) & 1][1][0], zc[(ct) & 1][1][1]), cvt_pk(zc[(ct) & 1][1][2], zc[(ct) & 1][1][3])}; \
    PIN1(zx[ct]); SB()
    f32x4 acc[6];
    {
      f32x4 zc[2][2];
      ZTILE(0);
      ZTILE(1);
      ZCVT(0);
#pragma unroll
      for (int j = 0; j < 6; ++j) { acc[j] = f32x4{0.f, 0.f, 0.f, 0.f}; PIN1(acc[j]); }
      SB();
      ZCVT(1);
    }
    STAMP(1);
    STAMP4(1);
    for (unsigned spins = 0;;) {
      unsigned mx = 0;
#pragma unroll
      for (int kk = 0; kk < 4; ++kk) {
        SMF_A(acc[0], mf[kk], wqkv[kk][0], sidx);
        mx = umax4(mx, mf[kk]);
        PIN1(mx);
        SB();
        SMF_A(acc[1], mf[kk], wqkv[kk][1], sidx);
        bool bad = false;
        if (kk == 3) { bad = __any(mx == SENT); SB(); }
#pragma unroll
        for (int j = 2; j < 6; ++j) SMF_A(acc[j], mf[kk], wqkv[kk][j], sidx);
        SB();
        if (kk == 0) STAMP4(2);
        if (kk == 3) mx = bad;
      }
      if (!mx) break;
      CNT(0);
      if (spin_fail(spins, ctl, dead)) break;
      PIN1(a_mem);
#pragma unroll
      for (int kk = 0; kk < 4; ++kk) mf[kk] = LD16(rs, a_mem, kk * 1024, so_m);
      NOPS(8);
#pragma unroll
      for (int j = 0; j < 6; ++j) { acc[j] = f32x4{0.f, 0.f, 0.f, 0.f}; PIN1(acc[j]); }
      NOPS(2);
      SB();
    }
    STAMP(2);
    STAMP4(3);
    {
      LAS unsigned char* rp = L + L_RED + w * 3456 + lg * 288 + l15 * 8;
#pragma unroll
      for (int m = 0; m < 3; ++m) {
        if (m == 0) NOPS(3);
        PIN2(acc[2 * m], acc[2 * m + 1]);
        *(LAS f32x2*)(rp + m * 1152) = f32x2{fadd(acc[2 * m][0], acc[2 * m][2]), fadd(acc[2 * m + 1][0], acc[2 * m + 1][2])};
        *(LAS f32x2*)(rp + m * 1152 + 144) = f32x2{fadd(acc[2 * m][1], acc[2 * m][3]), fadd(acc[2 * m + 1][1], acc[2 * m + 1][3])};
        SB();
      }
    }
    STAMP4(4);
    BAR();
    STAMP4(5);
    const int so_q = so_w0 + QKV_BASE + (s & 3) * QKV_SLOT;
    if (w < 3) {
      LAS f32x4* rr = (LAS f32x4*)(L + L_RED + w * 1152 + (l15 >> 1) * 144 + lg * 32 + (lane & 1) * 16);
      f32x4 x0 = rr[0], x1 = rr[216], x2 = rr[432], x3 = rr[648];
      f32x4 x = (x0 + x1) + (x2 + x3);
      __builtin_amdgcn_raw_buffer_store_b64(v2u{cvt_pk(x[0], x[1]), cvt_pk(x[2], x[3])}, rs, a_pub, so_q, AUX_SC1);
      SB();
    }
    STAMP(3);
    STAMP4(6);
    unsigned tq = 0, rbf = 0;
    v4u q2[3], tsv[2];
    int lw[3];
    if (w < 2) {
      if (w == 0) tq = rtc32();
#pragma unroll
      for (int i = 0; i < 3; ++i) { lw[i] = l_g2[i] + (par ? l_g2p[i] : 0); PIN1(lw[i]); }
      wait_sched(I2, 0);
      STAMP2(0);
#pragma unroll
      for (int i = 0; i < 3; ++i) q2[i] = LD16(rs, a_g2[i], 0, so_q);
      SB();
    }
    {
      f32x4 zc[2][2];
      ZTILE(2);
      ZTILE(3);
      ZCVT(2);
      LAS unsigned char* kb0 = vb0 + (L_KX - L_VX);
      LAS unsigned char* kb1 = vb1 + (L_KX - L_VX);
#pragma unroll
      for (int kt = 0; kt < 2; ++kt)
#pragma unroll
        for (int kk = 0; kk < 4; ++kk) kxf[kt][kk] = *(LAS v4u*)(((kk & 1) ? kb1 : kb0) + kt * 4096 + (kk >> 1) * 2048);
      SB();
      ZCVT(3);
    }
    if (w >= 2) {
      wait_sched(I2, PFLAG);
      pf_issue(min(s + 1, NSEG - 1));
      rbf = __hip_atomic_load(ctl + CTL_RB + 64 * (min(s + 2, NSEG - 1) >> 3), RLX_AGENT);
      if (w == 2) {
        const int so_r = so_w0 + QKV_BASE + ((s + 3) & 3) * QKV_SLOT;
        __builtin_amdgcn_raw_buffer_store_b128(sentv, rs, vo(cu * 1536 + lane * 16), so_r, AUX_SC1);
        __builtin_amdgcn_raw_buffer_store_b128(sentv, rs, lane < 32 ? vo(cu * 1536 + 1024 + lane * 16) : 0x40000000, so_r, AUX_SC1);
        __builtin_amdgcn_raw_buffer_store_b128(sentv, rs, a_mr, so_w0 + ((s + 2) & 3) * MEM_SLOT, AUX_SC1);
        __builtin_amdgcn_raw_buffer_store_b128(sentv, rs, a_ts, so_w0 + ((s + 1) & 3) * MEM_SLOT, AUX_SC1);
      } else {
        tsv[0] = LD16(rs, a_tg, 0, so_m);
        tsv[1] = LD16(rs, a_tg, 0, so_m + 16384);
        const int so_pr = so_part((s + 3) & 3);
        PIN1(a_pg);
#pragma unroll
        for (int k = 0; k < 4; ++k) __builtin_amdgcn_raw_buffer_store_b128(sentv, rs, a_pg + k * 1024, so_pr, AUX_SC1);
      }
      SB();
    }
    if (w < 2) {
      for (unsigned spins = 0;;) {
        unsigned mx = 0;
#pragma unroll
        for (int i = 0; i < 3; ++i) {
          *(LAS v4u*)(L + lw[i]) = q2[i];
          mx = umax4(mx, q2[i]);
          SB();
          if (i == 0) STAMP2(1);
        }
        if (!__any(mx == SENT)) break;
        CNT(1);
        if (spin_fail(spins, ctl, dead)) break;
#pragma unroll
        for (int i = 0; i < 3; ++i) q2[i] = LD16(rs, a_g2[i], 0, so_q);
        SB();
      }
    }
    STAMP(4);
    STAMP2(2);
    BAR();
    STAMP2(3);
    v4u bx;
    float rinv;
    const int so_p = so_part(s & 3);
    {
      v4u qf[4], kvf[4], vkf[4];
#pragma unroll
      for (int kk = 0; kk < 4; ++kk) {
        qf[kk] = *(LAS v4u*)(L + L_QF + par * 4096 + kk * 1024 + lane * 16);
        SB();
        kvf[kk] = *(LAS v4u*)(L + L_KVF + kk * 1024 + lane * 16);
        SB();
      }
#pragma unroll
      for (int kk = 0; kk < 4; ++kk) vkf[kk] = *(LAS v4u*)(L + L_KVF + kk * 1024 + (lane ^ 8) * 16);
      SB();
      f32x4 s0, s1, sm, vw[4];
      MF_C(s0, kxf[0][0], qf[0], msk[0]);
      MF_C(s1, kxf[1][0], qf[0], msk[1]);
      MF_C(sm, kvf[0], qf[0], msk[2]);
#pragma unroll
      for (int kk = 1; kk < 4; ++kk) {
        MF_A(s0, kxf[0][kk], qf[kk], "v");
        MF_A(s1, kxf[1][kk], qf[kk], "v");
        MF_A(sm, kvf[kk], qf[kk], "v");
      }
#define VW(i) do { if (((i) & 3) == 0) MF_Z(vw[(i) >> 2], vkf[0], wo[(i) >> 2][0], "a"); else MF_A(vw[(i) >> 2], vkf[(i) & 3], wo[(i) >> 2][(i) & 3], "a"); } while (0)
      STAMP2(4);
      VW(0); VW(1);
      NOPS(4);
      PIN1(s0);
      float mx = fmx(fmx(s0[0], s0[1]), s0[2]);
      PIN1(mx);
      SB();
      PIN1(s1);
      float mx2 = fmx(fmx(s1[1], s1[2]), s1[3]);
      mx = fmx(fmx(mx, s0[3]), s1[0]);
      PIN2(mx, mx2);
      SB();
      PIN1(sm);
      mx2 = fmx(fmx(mx2, sm[2]), sm[3]);
      mx = fmx(fmx(mx, sm[0]), sm[1]);
      SB();
      VW(2);
      mx = fmx(mx, mx2);
      SB();
      {
        auto r = __builtin_amdgcn_permlane16_swap(ubits(mx), ubits(mx), false, false);
        SB();
        VW(3);
        mx = fmx(fbits(r[0]), fbits(r[1]));
        auto r2 = __builtin_amdgcn_permlane32_swap(ubits(mx), ubits(mx), false, false);
        SB();
        VW(4);
        mx = fmx(fbits(r2[0]), fbits(r2[1]));
      }
      const float nm = -mx * CEXP;
      SB();
      STAMP2(5);
      float e0[4], e1[4], em[4];
      VW(5);
#pragma unroll
      for (int r = 0; r < 4; ++r) e0[r] = fmaf(s0[r], CEXP, nm);
      SB();
      VW(6);
      e0[0] = __builtin_amdgcn_exp2f(e0[0]);
      e1[0] = fmaf(s1[0], CEXP, nm);
      e1[1] = fmaf(s1[1], CEXP, nm);
      SB();
      VW(7);
      e0[1] = __builtin_amdgcn_exp2f(e0[1]);
      e1[2] = fmaf(s1[2], CEXP, nm);
      e1[3] = fmaf(s1[3], CEXP, nm);
      SB();
      VW(8);
      e0[2] = __builtin_amdgcn_exp2f(e0[2]);
      em[0] = fmaf(sm[0], CEXP, nm);
      em[1] = fmaf(sm[1], CEXP, nm);
      SB();
      VW(9);
      e0[3] = __builtin_amdgcn_exp2f(e0[3]);
      em[2] = fmaf(sm[2], CEXP, nm);
      em[3] = fmaf(sm[3], CEXP, nm);
      SB();
      VW(10);
      e1[0] = __builtin_amdgcn_exp2f(e1[0]);
      bx.x = cvt_pk(e0[0], e0[1]);
      SB();
      VW(11);
      e1[1] = __builtin_amdgcn_exp2f(e1[1]);
      bx.y = cvt_pk(e0[2], e0[3]);
      SB();
      VW(12);
      e1[2] = __builtin_amdgcn_exp2f(e1[2]);
      PIN1(vw[0]);
      v4u zm01, zm23;
      zm01.x = cvt_pk(vw[0][0], vw[0][1]);
      SB();
      VW(13);
      e1[3] = __builtin_amdgcn_exp2f(e1[3]);
      zm01.y = cvt_pk(vw[0][2], vw[0][3]);
      SB();
      VW(14);
      em[0] = __builtin_amdgcn_exp2f(em[0]);
      bx.z = cvt_pk(e1[0], e1[1]);
      SB();
      VW(15);
      em[1] = __builtin_amdgcn_exp2f(em[1]);
      bx.w = cvt_pk(e1[2], e1[3]);
      PIN1(bx);
      SB();
      STAMP2(6);
      em[2] = __builtin_amdgcn_exp2f(em[2]);
      em[3] = __builtin_amdgcn_exp2f(em[3]);
      PIN2(em[2], em[3]);
      SB();
      f32x4 ls, o[4];
      MF_Z(ls, ones, bx, "v");
      PIN1(vw[1]);
      zm01.z = cvt_pk(vw[1][0], vw[1][1]);
      bm.x = cvt_pk(em[0], em[1]);
      SB();
      MF_Z(o[0], zx[0], bx, "v");
      bm.y = cvt_pk(em[2], em[3]);
      zm01.w = cvt_pk(vw[1][2], vw[1][3]);
      PIN2(bm, zm01);
      SB();
      MF_Z(o[1], zx[1], bx, "v");
      bm2.z = bm.x;
      bm2.w = bm.y;
      PIN1(bm2);
      SB();
      MF_A(ls, ones, bm, "v");
      PIN1(vw[2]);
      zm23.x = cvt_pk(vw[2][0], vw[2][1]);
      zm23.y = cvt_pk(vw[2][2], vw[2][3]);
      SB();
      MF_A(o[0], zm01, bm, "v");
      PIN1(vw[3]);
      zm23.z = cvt_pk(vw[3][0], vw[3][1]);
      zm23.w = cvt_pk(vw[3][2], vw[3][3]);
      PIN1(zm23);
      SB();
      MF_A(o[1], zm01, bm2, "v");
      MF_Z(o[2], zx[2], bx, "v");
      MF_Z(o[3], zx[3], bx, "v");
      MF_A(o[2], zm23, bm, "v");
      MF_A(o[3], zm23, bm2, "v");
      PIN1(ls);
      rinv = __builtin_amdgcn_rcpf(ls[0]);
      PIN1(rinv);
      SB();
      PIN2(o[0], o[1]);
      v4u va = {cvt_pk(o[0][0] * rinv, o[0][1] * rinv), cvt_pk(o[0][2] * rinv, o[0][3] * rinv), cvt_pk(o[1][0] * rinv, o[1][1] * rinv), cvt_pk(o[1][2] * rinv, o[1][3] * rinv)};
      PIN1(va);
      SB();
      PIN2(o[2], o[3]);
      v4u vb = {cvt_pk(o[2][0] * rinv, o[2][1] * rinv), cvt_pk(o[2][2] * rinv, o[2][3] * rinv), cvt_pk(o[3][0] * rinv, o[3][1] * rinv), cvt_pk(o[3][2] * rinv, o[3][3] * rinv)};
#pragma unroll
      for (int r = 0; r < 4; ++r) va[r] = __builtin_amdgcn_update_dpp(va[r], vb[r], 0x118, 0xf, 0xc, false);
      __builtin_amdgcn_raw_buffer_store_b128(va, rs, a_ps, so_p, AUX_SC1);
      SB();
    }
    STAMP(5);
    STAMP2(7);
    v4u pg[4];
    unsigned tp = 0;
    if (w == 0) tp = rtc32();
    {
      const int q4 = l15 >> 2, p4 = lane & 3;
      f32x4 o[2];
      v4u ta[2], tb2[2];
#pragma unroll
      for (int j = 0; j < 2; ++j) {
        const int chunk = 2 * (2 * w + j) + (p4 >> 1);
        LAS unsigned char* vp = L + L_VX + par * 8192 + chunk * 256 + (((4 * lg + q4) ^ (chunk & 7)) * 16) + (p4 & 1) * 8;
        v2u t0 = trread(vp), t1 = trread(vp + 4096);
        v2u tm = trread(L + L_KVF + chunk * 256 + ((4 * lg + q4) ^ 8) * 16 + (p4 & 1) * 8);
        ta[j] = v4u{t0.x, t0.y, t1.x, t1.y};
        tb2[j] = v4u{tm.x, tm.y, t0.x, t0.y};
      }
      PIN2(ta[0], tb2[0]);
      PIN2(ta[1], tb2[1]);
      NOPS(2);
      MF_Z(o[0], ta[0], bx, "v");
      MF_Z(o[1], ta[1], bx, "v");
      MF_A(o[0], tb2[0], bm, "v");
      MF_A(o[1], tb2[1], bm, "v");
      NOPS(8);
      PIN2(o[0], o[1]);
      PIN1(a_ox);
#pragma unroll
      for (int j = 0; j < 2; ++j) {
        v2u ov = {cvt_pk(o[j][0] * rinv, o[j][1] * rinv), cvt_pk(o[j][2] * rinv, o[j][3] * rinv)};
        __builtin_amdgcn_raw_buffer_store_b64(ov, ro, a_ox + j * 32, s * 32 * 4096 * 2, OXAUX);
      }
      SB();
    }
    if (w == 0) {
      wait_sched(I3, 0);
      STAMP3(0);
      PIN1(a_pg);
#pragma unroll
      for (int k = 0; k < 4; ++k) pg[k] = LD16(rs, a_pg, k * 1024, so_p);
      SB();
    }
    if (w >= 2) {
      pf_write(s + 1);
      asm volatile("s_waitcnt vmcnt(0)" ::: "memory");
      rb_wait(__builtin_amdgcn_readfirstlane(rbf), min(s + 2, NSEG - 1) >> 3);
    }
    if (w == 3) {
      int n1 = 0, n2 = 0, n3 = 0;
      if (s >= 1) {
        for (unsigned spins = 0;;) {
          if (!__any(umax3(umax3(tsv[0].x, tsv[0].y, tsv[0].z), tsv[1].x, umax3(0u, tsv[1].y, tsv[1].z)) == SENT)) break;
          if (spin_fail(spins, ctl, dead)) break;
          tsv[0] = LD16(rs, a_tg, 0, so_m);
          tsv[1] = LD16(rs, a_tg, 0, so_m + 16384);
        }
        const int Tq = (int)wave_umax(max(tsv[0].x - tb, tsv[1].x - tb)), Tp = (int)wave_umax(max(tsv[0].y - tb, tsv[1].y - tb)),
                  Tm = (int)wave_umax(max(tsv[0].z - tb, tsv[1].z - tb));
        if (pTm + EPS1 - SAFE <= p1) { const int m = Tq - p1; d2 = imax(m, m2); m2 = m; }
        if (Tq + EPS2 - SAFE <= p2) { const int m = Tp - p2; d3 = imax(m, m3); m3 = m; }
        if (Tp + EPS3 - SAFE <= p3) { const int m = Tm - p3; d1 = imax(m, m1); m1 = m; }
        n1 = Tm + EPS1 - SAFE <= c1 ? c3 + d1 + EPS1 : imax(c1, Tm) + d1 + d2 + d3 + RELOCK;
        pTm = Tm;
        n2 = n1 + d2 + EPS2;
        n3 = n2 + d3 + EPS3;
      }
      p1 = c1; p2 = c2; p3 = c3;
      c1 = n1; c2 = n2; c3 = n3;
      if (lane == 0) *(LAS v4u*)sched = v4u{tb + (unsigned)n1, tb + (unsigned)n2, tb + (unsigned)n3, 0u};
    }
    if (w == 0 && s + 1 < NSEG) {
      STAMP3(1);
      f32x4 sum;
      for (unsigned spins = 0;;) {
        unsigned mx = umax4(0u, pg[0]);
        MF_Z(sum, sel, pg[0], "v");
#pragma unroll
        for (int k = 1; k < 4; ++k) {
          MF_A(sum, sel, pg[k], "v");
          mx = umax4(mx, pg[k]);
          PIN1(mx);
          SB();
          if (k == 1) STAMP3(2);
        }
        STAMP3(3);
        if (!__any(mx == SENT)) break;
        CNT(2);
        if (spin_fail(spins, ctl, dead)) break;
        PIN1(a_pg);
#pragma unroll
        for (int k = 0; k < 4; ++k) pg[k] = LD16(rs, a_pg, k * 1024, so_p);
        NOPS(4);
        SB();
      }
      NOPS(4);
      PIN1(sum);
#pragma unroll
      for (int r = 0; r < 4; ++r) sum[r] += fbits(__builtin_amdgcn_update_dpp(0u, ubits(sum[r]), 0x128, 0xf, 0xf, true));
      const int so_o = so_w0 + (s & 3) * MEM_SLOT;
      __builtin_amdgcn_raw_buffer_store_b64(v2u{cvt_pk(sum[0], sum[1]), cvt_pk(sum[2], sum[3])}, rs, a_ms, so_o, AUX_SC1);
      SB();
      const unsigned tm = rtc32();
      __builtin_amdgcn_raw_buffer_store_b128(v4u{min(tq, SENT - 1u), min(tp, SENT - 1u), min(tm, SENT - 1u), 0u}, rs, a_ts, so_o, AUX_SC1);
      SB();
    }
    STAMP(6);
    STAMP3(4);
    BAR();
    {
      const v4u sc = *(LAS v4u*)sched;
      I1 = __builtin_amdgcn_readfirstlane(sc.x);
      I2 = __builtin_amdgcn_readfirstlane(sc.y);
      I3 = __builtin_amdgcn_readfirstlane(sc.z);
    }
  }
}

#define GAUX AUX_SC1
#define NOLD 0
#define DMAPOL ""
#define FORCE_SAFE 0
#define AP1 1
#define AP2 3
#define AP3 4
#define AP4 5
#define AP5 7
#define ABL 0
#define GDMA0 1
#define GDMAS 4
#define GBAR 28
struct GItem {
  const u16* A; const u16* W; u16* C;
  int lda, ldw, ldc, nk;
  float scale;
  int flag;
};
constexpr int G_STAGE = 131072;
constexpr int GEMM_LDS = G_STAGE + 16384;

__device__ __forceinline__ f32x16 mfma32(v4u a, v4u b, f32x16 c) {
  return __builtin_amdgcn_mfma_f32_32x32x16_bf16(__builtin_bit_cast(bf16x8, a), __builtin_bit_cast(bf16x8, b), c, 0, 0, 0);
}
__device__ __forceinline__ void dma16(unsigned m0v, int voff, __amdgpu_buffer_rsrc_t rs, int soff) {
  asm volatile("s_mov_b32 m0, %0\n\ts_nop 0\n\tbuffer_load_dwordx4 %1, %2, %3 offen " DMAPOL " lds" ::"s"(m0v), "v"(voff), "s"(rs), "s"(soff) : "memory");
}
__device__ __forceinline__ __amdgpu_buffer_rsrc_t mk_rsrc(const void* p, int bytes) {
  const unsigned long long u = (unsigned long long)p;
  const unsigned lo = __builtin_amdgcn_readfirstlane((unsigned)u), hi = __builtin_amdgcn_readfirstlane((unsigned)(u >> 32));
  return __builtin_amdgcn_make_buffer_rsrc((void*)(((unsigned long long)hi << 32) | lo), 0, __builtin_amdgcn_readfirstlane(bytes), 0x00020000);
}

struct GStream {
  __amdgpu_buffer_rsrc_t ra0, ra1, rw;
  int va0[2], va1[2], vw[2];
  int sa0, sa1, sw;
  int ra0s, ra1s, rws;
};
struct GCtx {
  LAS unsigned char* L;
  unsigned ldsb;
  int lane, w;
  int aoff[2], boff[4];
};
__device__ __forceinline__ void gs_set_a0(GStream& g, const GItem& it, bool valid, int lane, int w) {
  g.ra0 = mk_rsrc(it.A, (valid && !NOLD) ? (255 * it.lda + it.nk * 64) * 2 : 0);
#pragma unroll
  for (int e = 0; e < 2; ++e) g.va0[e] = (32 * w + 8 * e + (lane >> 3)) * it.lda * 2 + (((lane & 7) ^ (4 * e + (lane >> 4))) * 16);
  g.sa0 = 0;
  g.ra0s = 32 * it.lda;
}
__device__ __forceinline__ void gs_set_rest(GStream& g, const GItem& it, bool valid, int lane, int w) {
  g.ra1 = mk_rsrc(it.A, (valid && !NOLD) ? (255 * it.lda + it.nk * 64) * 2 : 0);
#pragma unroll
  for (int e = 0; e < 2; ++e) g.va1[e] = (128 + 32 * w + 8 * e + (lane >> 3)) * it.lda * 2 + (((lane & 7) ^ (4 * e + (lane >> 4))) * 16);
  g.sa1 = 0;
  g.ra1s = 32 * it.lda;
  g.rw = mk_rsrc(it.W, (valid && !NOLD) ? ((it.nk * 64 - 1) * it.ldw + 256) * 2 : 0);
#pragma unroll
  for (int e = 0; e < 2; ++e) g.vw[e] = (16 * w + (lane >> 4)) * it.ldw * 2 + (((lane & 15) ^ ((lane >> 4) << 2) ^ (e << 1)) * 16);
  g.sw = 0;
  g.rws = 8 * it.ldw;
}
template <int KIND, int P>
__device__ __forceinline__ void g_dma(const GCtx& c, const GStream& g, int i) {
  constexpr unsigned slot = KIND < 2 ? (P * 2 + KIND) * 16384u : 65536u + (P * 2 + KIND - 2) * 16384u;
  const unsigned m0v = c.ldsb + slot + i * 1024u;
  if (KIND == 0) dma16(m0v, g.va0[i & 1], g.ra0, g.sa0 + (i >> 1) * g.ra0s);
  else if (KIND == 1) dma16(m0v, g.va1[i & 1], g.ra1, g.sa1 + (i >> 1) * g.ra1s);
  else dma16(m0v, g.vw[i >> 1], g.rw, g.sw + i * g.rws + (KIND - 2) * 256);
}
template <int H, int P>
__device__ __forceinline__ v4u g_rda(const GCtx& c, int mt, int ks) {
  return *(LAS v4u*)(c.L + c.aoff[ks] + (P * 2 + H) * 16384 + mt * 2048);
}
template <int H, int P>
__device__ __forceinline__ v4u g_rdb(const GCtx& c, int nt, int ks) {
  LAS unsigned char* p = c.L + c.boff[nt] + (P * 2 + H) * 16384 + ks * 8192;
  const v2u lo = trread(p), hi = trread(p + 1024);
  return v4u{lo.x, lo.y, hi.x, hi.y};
}
typedef f32x4 GAcc[4][4];
typedef v4u GFrag[4][2];
template <bool ZERO, int RK, int RP, int DK, int DP>
__device__ __forceinline__ void g_phase(const GCtx& c, const GStream& g, GAcc& acc, const GFrag& af, const GFrag& bf, GFrag& rf) {
#pragma unroll
  for (int n = 0; n < 32; ++n) {
    const int ks = n >> 4, nt = (n >> 2) & 3, mt = n & 3;
    if (ZERO && ks == 0) asm volatile("v_mfma_f32_16x16x32_bf16 %0, %1, %2, 0" : "=a"(acc[nt][mt]) : "v"(bf[nt][ks]), "v"(af[mt][ks]));
    else asm volatile("v_mfma_f32_16x16x32_bf16 %0, %1, %2, %0" : "+a"(acc[nt][mt]) : "v"(bf[nt][ks]), "v"(af[mt][ks]));
    if (n < 16 && (n & 1) == 0) {
      if (RK < 2) rf[(n >> 1) & 3][n >> 3] = g_rda<RK & 1, RP>(c, (n >> 1) & 3, n >> 3);
      else rf[(n >> 1) & 3][n >> 3] = g_rdb<RK & 1, RP>(c, (n >> 1) & 3, n >> 3);
    }
    if (n >= GDMA0 && n < GDMA0 + 4 * GDMAS && ((n - GDMA0) % GDMAS) == 0) g_dma<DK, DP>(c, g, (n - GDMA0) / GDMAS);
    if (n == GBAR) asm volatile("s_waitcnt vmcnt(24) lgkmcnt(0)\n\ts_barrier" ::: "memory");
    SB();
  }
}
template <bool FIRST>
__device__ __forceinline__ void g_pair(const GCtx& c, GStream& g, GAcc (&acc)[2][2], GFrag& a0, GFrag& a1, GFrag& bx, GFrag& by, bool sw, const GItem& nx,
                                       bool nvalid) {
  g_phase<FIRST, 3, 0, 2, 0>(c, g, acc[0][0], a0, bx, by);
  g_phase<FIRST, 1, 0, 3, 0>(c, g, acc[0][1], a0, by, a1);
  g.sw += 16 * g.rws;
  g_phase<FIRST, 0, 1, 1, 0>(c, g, acc[1][1], a1, by, a0);
  g.sa1 += 128;
  g_phase<FIRST, 2, 1, 0, 1>(c, g, acc[1][0], a1, bx, by);
  g.sa0 += 128;
  if (sw) gs_set_a0(g, nx, nvalid, c.lane, c.w);
  g_phase<false, 3, 1, 2, 1>(c, g, acc[0][0], a0, by, bx);
  g_phase<false, 1, 1, 3, 1>(c, g, acc[0][1], a0, bx, a1);
  g.sw += 16 * g.rws;
  g_phase<false, 0, 0, 1, 1>(c, g, acc[1][1], a1, bx, a0);
  g.sa1 += 128;
  g_phase<false, 2, 0, 0, 0>(c, g, acc[1][0], a1, by, bx);
  g.sa0 += 128;
  if (sw) gs_set_rest(g, nx, nvalid, c.lane, c.w);
}
template <int AUX>
__device__ __forceinline__ void g_epilogue(const GCtx& c, const GItem& it, GAcc (&acc)[2][2]) {
  int lane = c.lane;
  NOPS(8);
  PIN1(lane);
  const int m = lane & 15, gq = lane >> 4, wr = c.w >> 1, wc = c.w & 1;
  LAS unsigned char* st = c.L + G_STAGE + c.w * 4096;
  __amdgpu_buffer_rsrc_t rc = mk_rsrc(it.C, (255 * it.ldc + 256) * 2);
  const int vc = ((wr * 64 + (lane >> 3)) * it.ldc + wc * 64 + (lane & 7) * 8) * 2;
  const float sc = it.scale;
#pragma unroll
  for (int i = 0; i < 2; ++i)
#pragma unroll
    for (int j = 0; j < 2; ++j)
#pragma unroll
      for (int mh = 0; mh < 2; ++mh) {
#pragma unroll
        for (int mt = 2 * mh; mt < 2 * mh + 2; ++mt) {
          asm volatile("" : "+a"(acc[i][j][0][mt]), "+a"(acc[i][j][1][mt]), "+a"(acc[i][j][2][mt]), "+a"(acc[i][j][3][mt]));
#pragma unroll
          for (int nt = 0; nt < 4; ++nt) {
            const f32x4& v = acc[i][j][nt][mt];
            *(LAS v2u*)(st + ((mt & 1) * 16 + m) * 128 + (((nt * 2 + (gq >> 1)) ^ (m & 7)) * 16) + (gq & 1) * 8) =
                v2u{cvt_pk(v[0] * sc, v[1] * sc), cvt_pk(v[2] * sc, v[3] * sc)};
          }
        }
#pragma unroll
        for (int q = 0; q < 4; ++q) {
          const v4u o = *(LAS v4u*)(st + (8 * q + (lane >> 3)) * 128 + (((lane & 7) ^ (lane >> 3)) * 16));
          __builtin_amdgcn_raw_buffer_store_b128(o, rc, vc, ((i * 128 + mh * 32 + 8 * q) * it.ldc + j * 128) * 2, AUX);
        }
        SB();
      }
}
template <int AUX, class F>
__device__ __forceinline__ void gemm_tiles(LAS unsigned char* L, int n, F item, gu32* ctl) {
  if (n <= 0) return;
  GCtx c;
  c.L = L;
  c.lane = threadIdx.x & 63;
  c.w = __builtin_amdgcn_readfirstlane(threadIdx.x >> 6);
  c.ldsb = (unsigned)(size_t)L + c.w * 4096u;
  const int lane = c.lane, wr = c.w >> 1, wc = c.w & 1;
#pragma unroll
  for (int ks = 0; ks < 2; ++ks) c.aoff[ks] = (wr * 64 + (lane & 15)) * 128 + (((4 * ks + (lane >> 4)) ^ ((lane >> 1) & 7)) * 16);
#pragma unroll
  for (int nt = 0; nt < 4; ++nt)
    c.boff[nt] = 65536 + (8 * (lane >> 4) + ((lane >> 2) & 3)) * 256 +
                 (((8 * wc + 2 * nt + ((lane >> 1) & 1)) ^ (((lane >> 2) & 3) << 2) ^ (((lane >> 4) & 1) << 1)) * 16) + (lane & 1) * 8;
  GItem cur = item(0);
  GStream g;
  gs_set_a0(g, cur, true, lane, c.w);
  gs_set_rest(g, cur, true, lane, c.w);
  GFrag a0, a1, bx, by;
  GAcc acc[2][2];
  asm volatile("s_waitcnt vmcnt(0) lgkmcnt(0)\n\ts_barrier" ::: "memory");
#pragma unroll
  for (int i = 0; i < 4; ++i) g_dma<0, 0>(c, g, i);
#pragma unroll
  for (int i = 0; i < 4; ++i) g_dma<2, 0>(c, g, i);
#pragma unroll
  for (int i = 0; i < 4; ++i) g_dma<3, 0>(c, g, i);
#pragma unroll
  for (int i = 0; i < 4; ++i) g_dma<1, 0>(c, g, i);
  g.sa0 += 128; g.sa1 += 128; g.sw += 16 * g.rws;
#pragma unroll
  for (int i = 0; i < 4; ++i) g_dma<0, 1>(c, g, i);
#pragma unroll
  for (int i = 0; i < 4; ++i) g_dma<2, 1>(c, g, i);
#pragma unroll
  for (int i = 0; i < 4; ++i) g_dma<3, 1>(c, g, i);
#pragma unroll
  for (int i = 0; i < 4; ++i) g_dma<1, 1>(c, g, i);
  g.sa0 += 128; g.sa1 += 128; g.sw += 16 * g.rws;
  asm volatile("s_waitcnt vmcnt(24)\n\ts_barrier" ::: "memory");
#pragma unroll
  for (int k = 0; k < 8; ++k) { a0[k & 3][k >> 2] = g_rda<0, 0>(c, k & 3, k >> 2); bx[k & 3][k >> 2] = g_rdb<0, 0>(c, k & 3, k >> 2); }
  asm volatile("s_waitcnt lgkmcnt(0)\n\ts_barrier" ::: "memory");
#pragma unroll
  for (int i = 0; i < 4; ++i) g_dma<0, 0>(c, g, i);
  g.sa0 += 128;
  asm volatile("s_waitcnt vmcnt(24)\n\ts_barrier" ::: "memory");
  int pend = -1;
  for (int k = 0; k < n; ++k) {
    const bool nvalid = k + 1 < n;
    const GItem nx = item(nvalid ? k + 1 : k);
    g_pair<true>(c, g, acc, a0, a1, bx, by, cur.nk == 4, nx, nvalid);
    if (pend >= 0 && threadIdx.x == 0) __hip_atomic_fetch_add(ctl + pend, 1u, RLX_AGENT);
    for (int t = 2; t < cur.nk; t += 2) g_pair<false>(c, g, acc, a0, a1, bx, by, t == cur.nk - 4, nx, nvalid);
    g_epilogue<AUX>(c, cur, acc);
    pend = cur.flag;
    cur = nx;
  }
  asm volatile("s_waitcnt vmcnt(0) lgkmcnt(0)\n\ts_barrier" ::: "memory");
  if (pend >= 0 && threadIdx.x == 0) __hip_atomic_fetch_add(ctl + pend, 1u, RLX_AGENT);
}

#define ASTAMP(k)
#define MF32_C(d, a, b, c) asm volatile("v_mfma_f32_32x32x16_bf16 %0, %1, %2, %3" : "=&v"(d) : "v"(a), "v"(b), "v"(c))
#define MF32_V(d, a, b) asm volatile("v_mfma_f32_32x32x16_bf16 %0, %1, %2, %0" : "+v"(d) : "v"(a), "v"(b))
#define MF32_A(d, a, b) asm volatile("v_mfma_f32_32x32x16_bf16 %0, %1, %2, %0" : "+a"(d) : "v"(a), "v"(b))
struct ACtx {
  LAS unsigned char* L;
  unsigned ldsb;
  __amdgpu_buffer_rsrc_t rkv;
  int vk[2], vv;
  int koff[2], voff;
  v4u q[2][2], ones;
  v4u kf[2][2], vf[2][2], pb[2][2];
  f32x16 s[2], negr[2], o[2], l[2];
  float t[4];
};
__device__ __forceinline__ void a_dma(ACtx& c, int stage, int i) {
  const unsigned m0v = c.ldsb + (stage & 3) * 32768 + (i >> 2) * 16384 + (i & 3) * 1024;
  dma16(m0v, i < 4 ? c.vk[i & 1] : c.vv, c.rkv, stage * (128 * 6144) + (i < 4 ? (i >> 1) * 16 * 6144 : 2048 + (i & 3) * 8 * 6144));
}
__device__ __forceinline__ v4u a_rdk(ACtx& c, int ko, int blk) { return *(LAS v4u*)(c.L + ko + blk * 4096); }
__device__ __forceinline__ v4u a_rdv(ACtx& c, int vo_, int blk, int s) {
  LAS unsigned char* p = c.L + vo_ + blk * 4096 + s * 2048;
  const v2u lo = trread(p), hi = trread(p + 1024);
  return v4u{lo.x, lo.y, hi.x, hi.y};
}
template <int QT>
__device__ __forceinline__ void a_ref(ACtx& c) {
  float mx = c.s[QT][0];
#pragma unroll
  for (int r = 1; r < 16; ++r) mx = fmx(mx, c.s[QT][r]);
  auto sw = __builtin_amdgcn_permlane32_swap(ubits(mx), ubits(mx), false, false);
  const float d = fmx(fbits(sw[0]), fbits(sw[1]));
  c.negr[QT] -= d;
  c.s[QT] -= d;
  asm volatile("s_nop 1" : "+v"(c.negr[QT]), "+v"(c.s[QT]));
}
template <int QT, int BLK, int MODE>
__device__ __forceinline__ void a_tile(ACtx& c, int k0, int k1, int v0, int dstage) {
  constexpr int P = QT ^ 1, KN = (BLK + QT) & 1, V1 = (BLK + QT + 1) & 1, V2 = (BLK + 1) & 1;
  constexpr bool H1 = MODE & 1, H2 = MODE & 2, H0 = MODE & 4;
  float e[16];
  PIN1(c.s[QT]);
#pragma unroll
  for (int i = 0; i < 8; ++i) {
    if (i == 0 && H2) MF32_A(c.o[QT], c.vf[V2][1], c.pb[QT][1]);
    if (i == AP1 && H2 && !(ABL & 2)) MF32_A(c.l[QT], c.ones, c.pb[QT][1]);
    if (i == AP2 && H0) MF32_C(c.s[P], c.kf[KN][0], c.q[P][0], c.negr[P]);
    if (i == AP3 && H0) MF32_V(c.s[P], c.kf[KN][1], c.q[P][1]);
    if (i == AP4 && H1) MF32_A(c.o[P], c.vf[V1][0], c.pb[P][0]);
    if (i == AP5 && H1 && !(ABL & 2)) MF32_A(c.l[P], c.ones, c.pb[P][0]);
    SB();
    if (H0) { e[2 * i] = __builtin_amdgcn_exp2f(c.s[QT][2 * i]); if (i >= 6) PIN1(e[2 * i]); SB(); }
    if (i >= 2) { if (H0) c.pb[QT][(i - 2) >> 2][(i - 2) & 3] = cvt_pk(e[2 * i - 4], e[2 * i - 3]); }
    else if (H1) c.pb[P][1][2 + i] = cvt_pk(c.t[2 * i], c.t[2 * i + 1]);
    SB();
    if (H0) { e[2 * i + 1] = __builtin_amdgcn_exp2f(c.s[QT][2 * i + 1]); if (i >= 6) PIN1(e[2 * i + 1]); SB(); }
    if (QT == 0 && H0) {
      if (i == 2) c.kf[(BLK + 1) & 1][0] = a_rdk(c, k0, (BLK + 1) & 3);
      if (i == 4) c.kf[(BLK + 1) & 1][1] = a_rdk(c, k1, (BLK + 1) & 3);
      if (i == 6) c.vf[BLK & 1][0] = a_rdv(c, v0, BLK, 0);
    }
    if (QT == 1 && H0) {
      if (i == 2) c.vf[BLK & 1][1] = a_rdv(c, v0, BLK, 1);
    }
    if (H0 && i == (QT ? 6 : 1) && !(ABL & 16)) a_dma(c, dstage, 2 * BLK + QT);
    SB();
  }
  if (H0) {
#pragma unroll
    for (int j = 0; j < 4; ++j) c.t[j] = e[12 + j];
  }
}
template <int AUX>
__device__ __forceinline__ void a_store(u16* O, const f32x16 (&o)[2], const f32x16 (&l)[2], int el) {
  __amdgpu_buffer_rsrc_t ro = mk_rsrc(O, 2048 * 1024 * 2);
#pragma unroll
  for (int qt = 0; qt < 2; ++qt) {
    const float ri = __builtin_amdgcn_rcpf(l[qt][0]);
    unsigned d[8];
#pragma unroll
    for (int i = 0; i < 8; ++i) d[i] = cvt_pk(o[qt][2 * i] * ri, o[qt][2 * i + 1] * ri);
#pragma unroll
    for (int k = 0; k < 2; ++k) {
      auto x = __builtin_amdgcn_permlane32_swap(d[4 * k], d[4 * k + 2], false, false);
      auto y = __builtin_amdgcn_permlane32_swap(d[4 * k + 1], d[4 * k + 3], false, false);
      __builtin_amdgcn_raw_buffer_store_b128(v4u{x[0], y[0], x[1], y[1]}, ro, (el + qt * 32 * 1024 + k * 16) * 2, 0, AUX);
    }
  }
}
__device__ __forceinline__ void attn_safe(ACtx& c, f32x16 (&o)[2], f32x16 (&l)[2]) {
  float m[2] = {-3.0e38f, -3.0e38f};
#pragma unroll
  for (int qt = 0; qt < 2; ++qt)
#pragma unroll
    for (int r = 0; r < 16; ++r) { o[qt][r] = 0.f; l[qt][r] = 0.f; }
  for (int b = 0; b < 64; ++b) {
    if ((b & 3) == 0) {
      asm volatile("s_waitcnt lgkmcnt(0)\n\ts_barrier" ::: "memory");
      for (int i = 0; i < 8; ++i) {
        const unsigned m0v = c.ldsb + (i >> 2) * 16384 + (i & 3) * 1024;
        dma16(m0v, i < 4 ? ((i & 1) ? c.vk[1] : c.vk[0]) : c.vv, c.rkv, (b >> 2) * (128 * 6144) + (i < 4 ? (i >> 1) * 16 * 6144 : 2048 + (i & 3) * 8 * 6144));
      }
      asm volatile("s_waitcnt vmcnt(0)\n\ts_barrier" ::: "memory");
    }
    const v4u ka = a_rdk(c, c.koff[0], b & 3), kb = a_rdk(c, c.koff[1], b & 3), va = a_rdv(c, c.voff, b & 3, 0), vb = a_rdv(c, c.voff, b & 3, 1);
#pragma unroll
    for (int qt = 0; qt < 2; ++qt) {
      f32x16 s;
#pragma unroll
      for (int r = 0; r < 16; ++r) s[r] = 0.f;
      s = mfma32(kb, c.q[qt][1], mfma32(ka, c.q[qt][0], s));
      float mx = m[qt];
#pragma unroll
      for (int r = 0; r < 16; ++r) mx = fmx(mx, s[r]);
      auto sw = __builtin_amdgcn_permlane32_swap(ubits(mx), ubits(mx), false, false);
      mx = fmx(fbits(sw[0]), fbits(sw[1]));
      const float f = __builtin_amdgcn_exp2f(m[qt] - mx);
      m[qt] = mx;
      v4u p[2];
#pragma unroll
      for (int i = 0; i < 8; ++i) p[i >> 2][i & 3] = cvt_pk(__builtin_amdgcn_exp2f(s[2 * i] - mx), __builtin_amdgcn_exp2f(s[2 * i + 1] - mx));
      o[qt] = mfma32(vb, p[1], mfma32(va, p[0], o[qt] * f));
      l[qt] = mfma32(c.ones, p[1], mfma32(c.ones, p[0], l[qt] * f));
    }
  }
}
template <int AUX>
__device__ __forceinline__ void attn_item(LAS unsigned char* L, const u16* QKV, u16* O, int hp, int qb) {
  ACtx c;
  ASTAMP(0);
  int lane = threadIdx.x & 63;
  PIN1(lane);
  const int w = __builtin_amdgcn_readfirstlane(threadIdx.x >> 6), hs = w >> 1, hh = lane >> 5;
  c.L = L;
  c.ldsb = (unsigned)(size_t)L + w * 4096u;
  c.rkv = mk_rsrc(QKV + 1024 + hp * 64, (2047 * 3072 + 1024 + 64) * 2);
#pragma unroll
  for (int e = 0; e < 2; ++e) c.vk[e] = (32 * w + 8 * e + (lane >> 3)) * 6144 + (((lane & 7) ^ (4 * e + (lane >> 4))) * 16);
  c.vv = (32 * w + (lane >> 3)) * 6144 + (((lane & 7) ^ (((lane >> 4) & 1) << 2)) * 16);
#pragma unroll
  for (int ks = 0; ks < 2; ++ks) c.koff[ks] = (lane & 31) * 128 + (((hs * 4 + 2 * ks + hh) ^ ((lane >> 1) & 7)) * 16);
  c.voff = 16384 + (4 * hh + ((lane >> 2) & 3)) * 128 + (((hs * 4 + ((lane >> 4) & 1) * 2 + ((lane >> 1) & 1)) ^ (((lane >> 3) & 1) << 2)) * 16) + (lane & 1) * 8;
  asm volatile("s_waitcnt vmcnt(0) lgkmcnt(0)\n\ts_barrier" ::: "memory");
  const int q0 = qb * 128 + (w & 1) * 64;
#pragma unroll
  for (int qt = 0; qt < 2; ++qt)
#pragma unroll
    for (int ks = 0; ks < 2; ++ks) c.q[qt][ks] = *(const GAS v4u*)(QKV + (size_t)(q0 + qt * 32 + (lane & 31)) * 3072 + hp * 64 + hs * 32 + ks * 16 + hh * 8);
  SB();
#pragma unroll
  for (int st = 0; st < 2; ++st)
#pragma unroll
    for (int i = 0; i < 8; ++i) a_dma(c, st, i);
  c.ones = v4u{0x3F803F80u, 0x3F803F80u, 0x3F803F80u, 0x3F803F80u};
#pragma unroll
  for (int qt = 0; qt < 2; ++qt) {
#pragma unroll
    for (int r = 0; r < 16; ++r) { c.negr[qt][r] = 0.f; c.o[qt][r] = 0.f; c.l[qt][r] = 0.f; }
    asm volatile("" : "+v"(c.negr[qt]), "+a"(c.o[qt]), "+a"(c.l[qt]));
  }
  PIN1(c.ones);
  asm volatile("s_waitcnt vmcnt(8)\n\ts_barrier" : "+v"(c.q[0][0]), "+v"(c.q[0][1]), "+v"(c.q[1][0]), "+v"(c.q[1][1])::"memory");
  c.kf[0][0] = a_rdk(c, c.koff[0], 0);
  c.kf[0][1] = a_rdk(c, c.koff[1], 0);
  MF32_C(c.s[1], c.kf[0][0], c.q[1][0], c.negr[1]);
  MF32_V(c.s[1], c.kf[0][1], c.q[1][1]);
  MF32_C(c.s[0], c.kf[0][0], c.q[0][0], c.negr[0]);
  MF32_V(c.s[0], c.kf[0][1], c.q[0][1]);
  NOPS(12);
  PIN2(c.s[0], c.s[1]);
  a_ref<1>(c);
  a_ref<0>(c);
  ASTAMP(1);
  a_tile<0, 0, 4>(c, c.koff[0], c.koff[1], c.voff, 2);
  a_tile<1, 0, 5>(c, c.koff[0], c.koff[1], c.voff, 2);
  auto six = [&](int j) {
    const int so = (j & 3) * 32768, sn = ((j + 1) & 3) * 32768, ds = min(j + 2, 15);
    const int k0 = c.koff[0] + so, k1 = c.koff[1] + so, v0 = c.voff + so;
    a_tile<0, 1, 7>(c, k0, k1, v0, ds);
    a_tile<1, 1, 7>(c, k0, k1, v0, ds);
    a_tile<0, 2, 7>(c, k0, k1, v0, ds);
    if (!(ABL & 8)) asm volatile("s_waitcnt vmcnt(5)\n\ts_barrier" ::: "memory");
    a_tile<1, 2, 7>(c, k0, k1, v0, ds);
    a_tile<0, 3, 7>(c, c.koff[0] + sn, c.koff[1] + sn, v0, ds);
    a_tile<1, 3, 7>(c, k0, k1, v0, ds);
  };
  for (int j = 0; j < 15; ++j) {
    six(j);
    const int sn = ((j + 1) & 3) * 32768;
    a_tile<0, 0, 7>(c, c.koff[0] + sn, c.koff[1] + sn, c.voff + sn, min(j + 3, 15));
    a_tile<1, 0, 7>(c, c.koff[0] + sn, c.koff[1] + sn, c.voff + sn, min(j + 3, 15));
  }
  six(15);
  a_tile<0, 0, 3>(c, 0, 0, 0, 0);
  NOPS(2);
  MF32_A(c.o[1], c.vf[1][1], c.pb[1][1]);
  MF32_A(c.l[1], c.ones, c.pb[1][1]);
  NOPS(12);
  asm volatile("" : "+a"(c.o[0]), "+a"(c.l[0]), "+a"(c.o[1]), "+a"(c.l[1]), "+v"(c.s[0]), "+v"(c.negr[0]), "+v"(c.negr[1]));
  ASTAMP(2);
  bool bad = false;
#pragma unroll
  for (int qt = 0; qt < 2; ++qt) {
    float m = c.l[qt][0];
#pragma unroll
    for (int r = 0; r < 16; ++r) m = fmx(m, __builtin_fabsf(c.o[qt][r]));
    bad |= !(m < 1e30f);
  }
  LAS unsigned* vote = (LAS unsigned*)(L + G_STAGE);
  if (lane == 0) vote[w] = 0u;
  if (bad) vote[w] = 1u;
  asm volatile("s_waitcnt vmcnt(0) lgkmcnt(0)\n\ts_barrier" ::: "memory");
  const v4u vt = *(LAS v4u*)vote;
  if (FORCE_SAFE || __builtin_amdgcn_readfirstlane(vt.x | vt.y | vt.z | vt.w)) attn_safe(c, c.o, c.l);
  a_store<AUX>(O, c.o, c.l, (q0 + (lane & 31)) * 1024 + hp * 64 + hs * 32 + hh * 8);
  ASTAMP(3);
}

struct Args {
  const u16* Ax; const u16* Wq; const u16* Wk; const u16* Wv; const u16* Wo; const u16* Bx; const u16* Wb; const u16* Wob;
  u16* out; unsigned char* base;
};
constexpr float CB = 0.17677669529663687f * 1.4426950408889634f;
__device__ __forceinline__ GItem proj_item(const Args& a, int x, int idx) {
  GItem it;
  it.lda = 1024; it.nk = 16; it.scale = 1.f;
  const bool isb = (idx >= 60 && idx < 64) || (idx >= 76 && idx < 96);
  if (!isb) {
    const int j = idx < 60 ? idx : idx < 76 ? idx - 4 : idx - 24;
    const int rb = j / 12, ct = (j % 12) * 4 + x, m = ct >> 4;
    it.A = a.Ax + (size_t)rb * 256 * 1024;
    it.W = (m == 0 ? a.Wq : m == 1 ? a.Wk : a.Wv) + (ct & 15) * 256;
    it.ldw = 4096;
    it.C = (u16*)(a.base + XQKV_OFF) + (size_t)rb * 256 * LDX + ct * 256;
    it.ldc = LDX;
    it.flag = CTL_RB + 64 * rb;
  } else {
    const int j = idx < 64 ? idx - 60 : idx - 72;
    const int rb = j / 3, ct = (j % 3) * 4 + x;
    it.A = a.Bx + (size_t)rb * 256 * 1024;
    it.W = a.Wb + ct * 256;
    it.ldw = 3072;
    it.C = (u16*)(a.base + BQKV_OFF) + (size_t)rb * 256 * 3072 + ct * 256;
    it.ldc = 3072;
    if (ct < 4) it.scale = CB;
    it.flag = CTL_B;
  }
  return it;
}
__device__ __forceinline__ void wg_wait(gu32* ctl, int word, unsigned want) {
  if (threadIdx.x < 64) {
    bool dead = false;
    for (unsigned spins = 0; __builtin_amdgcn_readfirstlane(__hip_atomic_load(ctl + word, RLX_AGENT)) < want;) {
      __builtin_amdgcn_s_sleep(16);
      if (spin_fail(spins, ctl, dead)) break;
    }
    __builtin_amdgcn_fence(__ATOMIC_ACQUIRE, "agent");
    asm volatile("s_waitcnt vmcnt(0)" ::: "memory");
  }
  asm volatile("s_barrier" ::: "memory");
}
__device__ __forceinline__ int wg_next(LAS unsigned char* L, gu32* ctl, int done, int queue) {
  LAS int* slot = (LAS int*)(L + G_STAGE + 64);
  asm volatile("s_waitcnt vmcnt(0) lgkmcnt(0)\n\ts_barrier" ::: "memory");
  if (threadIdx.x == 0) {
    if (done >= 0) __hip_atomic_fetch_add(ctl + done, 1u, RLX_AGENT);
    *slot = (int)__hip_atomic_fetch_add(ctl + queue, 1u, RLX_AGENT);
  }
  asm volatile("s_waitcnt vmcnt(0) lgkmcnt(0)\n\ts_barrier" ::: "memory");
  return __builtin_amdgcn_readfirstlane(*slot);
}
#define OSTAMP(k)
__device__ __forceinline__ void others_role(const Args& a, LAS unsigned char* L, int x, int i) {
  gu32* ctl = (gu32*)(a.base + CTL_OFF);
  gemm_tiles<AUX_SC1>(L, i < 24 ? 4 : 3, [&](int k) { return proj_item(a, x, i + k * 32); }, ctl);
  OSTAMP(1);
  wg_wait(ctl, CTL_B, 96u);
  int done = -1;
  for (int q = 0; q < 4; ++q) {
    const int xq = (x + q) & 3;
    for (;;) {
      const int k = wg_next(L, ctl, done, CTL_AQ + 64 * xq);
      done = -1;
      if (k >= 64) break;
      attn_item<AUX_SC1>(L, (const u16*)(a.base + BQKV_OFF), (u16*)(a.base + BO_OFF), xq * 4 + (k & 3), k >> 2);
      done = CTL_ATT + 64 * (k >> 3);
    }
  }
  OSTAMP(2);
  const int k = wg_next(L, ctl, -1, CTL_GQ);
  if (k < 32) {
    wg_wait(ctl, CTL_ATT + 64 * (k >> 2), 32u);
    gemm_tiles<0>(L, 1, [&](int) {
      GItem it;
      it.A = (const u16*)(a.base + BO_OFF) + (size_t)(k >> 2) * 256 * 1024; it.lda = 1024;
      it.W = a.Wob + (k & 3) * 256; it.ldw = 1024;
      it.C = a.out + (size_t)(k >> 2) * 256 * 2048 + 1024 + (k & 3) * 256; it.ldc = 2048;
      it.nk = 16; it.scale = 1.f; it.flag = -1;
      return it; }, ctl);
  }
  OSTAMP(3);
}
__global__ void __launch_bounds__(256, 1) k_main(Args a) {
  extern __shared__ __attribute__((aligned(16))) unsigned char lds[];
  const int b = blockIdx.x;
  OSTAMP(0);
  if ((b & 7) < 4) {
    ChainArgs ca{a.Wq, a.Wk, a.Wv, a.Wo, a.base};
    chain_role(ca, (LAS unsigned char*)lds, (b >> 3) * 4 + (b & 3));
    OSTAMP(3);
  } else {
    others_role(a, (LAS unsigned char*)lds, b & 3, b >> 3);
  }
}
__global__ void __launch_bounds__(256, 1) k_outA(Args a) {
  extern __shared__ __attribute__((aligned(16))) unsigned char lds[];
  const int b = blockIdx.x, kq = b & 7, t = b >> 3;
  gemm_tiles<0>((LAS unsigned char*)lds, 1, [&](int) {
    GItem it;
    it.A = (const u16*)(a.base + OX_OFF) + (size_t)(t >> 2) * 256 * 4096 + kq * 512; it.lda = 4096;
    it.W = a.Wo + (size_t)kq * 512 * 1024 + (t & 3) * 256; it.ldw = 1024;
    it.C = (u16*)(a.base + SLAB_OFF) + (size_t)kq * SEQ * 1024 + (size_t)(t >> 2) * 256 * 1024 + (t & 3) * 256; it.ldc = 1024;
    it.nk = 8; it.scale = 1.f; it.flag = -1;
    return it; }, (gu32*)(a.base + CTL_OFF));
}
__global__ void __launch_bounds__(256) k_sum(Args a) {
  const int i = blockIdx.x * 256 + threadIdx.x;
  const GAS v4u* s = (const GAS v4u*)(a.base + SLAB_OFF) + i;
  v4u v[8];
#pragma unroll
  for (int k = 0; k < 8; ++k) v[k] = s[(size_t)k * SEQ * 128];
  v4u o;
#pragma unroll
  for (int j = 0; j < 4; ++j) {
    float lo = 0.f, hi = 0.f;
#pragma unroll
    for (int k = 0; k < 8; ++k) { lo += fbits(v[k][j] << 16); hi += fbits(v[k][j] & 0xFFFF0000u); }
    o[j] = cvt_pk(lo, hi);
  }
  *(GAS v4u*)(a.out + (size_t)(i >> 7) * 2048 + (i & 127) * 8) = o;
}

}

extern "C" void kernel_launch(void* const* d_in, const int* in_sizes, int n_in, void* d_out, int out_size, void* d_ws, size_t ws_size,
                              hipStream_t stream) {
  unsigned char* base = (unsigned char*)(((uintptr_t)d_ws + (2u << 20) - 1) & ~(uintptr_t)((2u << 20) - 1));
  Args a{(const u16*)d_in[0], (const u16*)d_in[2], (const u16*)d_in[3], (const u16*)d_in[4], (const u16*)d_in[5],
         (const u16*)d_in[7], (const u16*)d_in[8], (const u16*)d_in[9], (u16*)d_out, base};
  (void)hipFuncSetAttribute((const void*)k_main, hipFuncAttributeMaxDynamicSharedMemorySize, GEMM_LDS);
  (void)hipFuncSetAttribute((const void*)k_outA, hipFuncAttributeMaxDynamicSharedMemorySize, GEMM_LDS);
  k_init<<<256, 256, 0, stream>>>(base, (const u16*)d_in[1]);
  k_main<<<256, 256, GEMM_LDS, stream>>>(a);
  k_outA<<<256, 256, GEMM_LDS, stream>>>(a);
  k_sum<<<1024, 256, 0, stream>>>(a);
}
```

```cpp
#include <hip/hip_runtime.h>
#include <hip/hip_bf16.h>
#include <stdint.h>
#include <stdio.h>
#include <type_traits>

#define LAS __attribute__((address_space(3)))
#define GAS __attribute__((address_space(1)))
typedef unsigned v4u __attribute__((ext_vector_type(4)));
typedef unsigned v2u __attribute__((ext_vector_type(2)));
typedef unsigned v8u __attribute__((ext_vector_type(8)));
typedef float f32x4 __attribute__((ext_vector_type(4)));
typedef float f32x2 __attribute__((ext_vector_type(2)));
typedef float f32x16 __attribute__((ext_vector_type(16)));
typedef __bf16 bf16x8 __attribute__((ext_vector_type(8)));
typedef __bf16 bf16x16 __attribute__((ext_vector_type(16)));
typedef __bf16 bf16x2 __attribute__((ext_vector_type(2)));
typedef short v4i16 __attribute__((ext_vector_type(4)));
typedef unsigned short u16;
typedef GAS unsigned gu32;
#define RLX_AGENT __ATOMIC_RELAXED, __HIP_MEMORY_SCOPE_AGENT

#define DBG_STAMP 0

namespace {

constexpr int SEQ = 2048, DM = 1024, NH = 32, DH = 128, NSEG = 64;
constexpr int LDX = 3 * NH * DH;
constexpr size_t COMM_BYTES = 6u << 20;
constexpr size_t CTL_OFF = COMM_BYTES;
constexpr size_t XQKV_OFF = CTL_OFF + (2u << 20);
constexpr size_t OX_OFF = XQKV_OFF + (size_t)SEQ * LDX * 2;
constexpr size_t BQKV_OFF = OX_OFF + (size_t)SEQ * 4096 * 2;
constexpr size_t BO_OFF = BQKV_OFF + (size_t)SEQ * 3072 * 2;
constexpr size_t SLAB_OFF = BO_OFF + (size_t)SEQ * 1024 * 2 + 4096;
constexpr size_t WS_END = SLAB_OFF + (size_t)8 * SEQ * 1024 * 2;
constexpr int CTL_POL = 0, CTL_ABORT = 64, CTL_RB = 128  , CTL_B = 1152, CTL_ATT = 1216  , CTL_AQ = 1728  , CTL_GQ = 1984, CTL_OT = 2048  , CTL_OX = 4096  ,
              CTL_O4 = 5632  , CTL_OQ = 7168, CTL_WORDS = 7232;
constexpr int NRB1 = 6;
constexpr int CTL_STAMP = 16384;
constexpr float CEXP = 0.08838834764831845f * 1.4426950408889634f;
#define FORCE_SAFE 0
constexpr unsigned SENT = 0xFFFFFFFFu;
constexpr int AUX_SC1 = 16;
constexpr unsigned SPIN_CAP = 1u << 15;
constexpr int MEM_SLOT = 2 * 65536, QKV_BASE = 2 * 262144, QKV_SLOT = 2 * 196608;

__device__ __forceinline__ int vo(int o) { return ((o >> 13) << 14) | (o & 8191); }
__device__ __forceinline__ unsigned cvt_pk(float lo, float hi) {
  f32x2 v = {lo, hi};
  return __builtin_bit_cast(unsigned, __builtin_convertvector(v, bf16x2));
}
__device__ __forceinline__ f32x4 mfma16(v4u a, v4u b, f32x4 c) {
  return __builtin_amdgcn_mfma_f32_16x16x32_bf16(__builtin_bit_cast(bf16x8, a), __builtin_bit_cast(bf16x8, b), c, 0, 0, 0);
}
__device__ __forceinline__ f32x4 smfmac(v4u a, v8u b, f32x4 c, int idx) {
  return __builtin_amdgcn_smfmac_f32_16x16x64_bf16(__builtin_bit_cast(bf16x8, a), __builtin_bit_cast(bf16x16, b), c, idx, 0, 0);
}
#define MF_Z(d, a, b, BC) asm volatile("v_mfma_f32_16x16x32_bf16 %0, %1, %2, 0" : "=&v"(d) : "v"(a), BC(b))
#define MF_C(d, a, b, c) asm volatile("v_mfma_f32_16x16x32_bf16 %0, %1, %2, %3" : "=&v"(d) : "v"(a), "v"(b), "v"(c))
#define MF_A(d, a, b, BC) asm volatile("v_mfma_f32_16x16x32_bf16 %0, %1, %2, %0" : "+v"(d) : "v"(a), BC(b))
#define SMF_A(d, a, b, i) asm volatile("v_smfmac_f32_16x16x64_bf16 %0, %1, %2, %3" : "+v"(d) : "v"(a), "a"(b), "v"(i))
#define NOPS(n) asm volatile("s_nop %0" ::"i"((n)-1))
#define PIN1(a) asm volatile("" : "+v"(a))
#define PIN2(a, b) asm volatile("" : "+v"(a), "+v"(b))
__device__ __forceinline__ v2u trread(LAS unsigned char* p) {
  return __builtin_bit_cast(v2u, __builtin_amdgcn_ds_read_tr16_b64_v4i16((LAS v4i16*)p));
}
__device__ __forceinline__ unsigned umax3(unsigned a, unsigned b, unsigned c) { return max(max(a, b), c); }
__device__ __forceinline__ unsigned umax4(unsigned m, v4u v) { return umax3(umax3(m, v.x, v.y), v.z, v.w); }
__device__ __forceinline__ float fbits(unsigned u) { return __builtin_bit_cast(float, u); }
__device__ __forceinline__ unsigned ubits(float f) { return __builtin_bit_cast(unsigned, f); }
#define SB() __builtin_amdgcn_sched_barrier(0)
#define BAR() asm volatile("s_waitcnt lgkmcnt(0)\n\ts_barrier" ::: "memory")
__device__ __forceinline__ unsigned rtc32_() { return (unsigned)__builtin_amdgcn_s_memrealtime(); }
__device__ __forceinline__ unsigned clk32() {
  unsigned long long t;
  asm volatile("s_memtime %0\n\ts_waitcnt lgkmcnt(0)" : "=s"(t)::"memory");
  return (unsigned)t;
}

__global__ void __launch_bounds__(256) k_init(unsigned char* base, const u16* mem0) {
  __amdgpu_buffer_rsrc_t rs = __builtin_amdgcn_make_buffer_rsrc(base, 0, (int)(COMM_BYTES + 65536), 0x00020000);
  const v4u s = {SENT, SENT, SENT, SENT};
  const int per = (int)(COMM_BYTES / 256);
  for (int o = blockIdx.x * per + threadIdx.x * 16; o < (int)(blockIdx.x + 1) * per; o += 4096) {
    v4u v = s;
    const int u = o - 3 * MEM_SLOT;
    if (u >= 0 && u < 32768) {
      const int lo = ((u >> 14) << 13) | (u & 8191);
      const int r = lo >> 7, rp = (lo >> 4) & 7;
      v = *(const GAS v4u*)(mem0 + (4 * (r & 1) + 2 * (rp >> 2) + (rp & 1)) * DM + 16 * (r >> 1) + 8 * ((rp >> 1) & 1));
      v = v4u{min(v.x, SENT - 1u), min(v.y, SENT - 1u), min(v.z, SENT - 1u), min(v.w, SENT - 1u)};
    }
    __builtin_amdgcn_raw_buffer_store_b128(v, rs, o, 0, AUX_SC1);
  }
  if (blockIdx.x == 255 && threadIdx.x < CTL_WORDS / 64)
    __hip_atomic_store((gu32*)(base + CTL_OFF) + threadIdx.x * 64, threadIdx.x == 0 ? 2u : 0u, RLX_AGENT);
}

struct ChainArgs {
  const u16* Wq; const u16* Wk; const u16* Wv; const u16* Wo;
  unsigned char* base;
};
constexpr int L_RED = 0;
constexpr int L_SCHED = 13824;
constexpr int L_KVF = 14336;
constexpr int SEG_B = 20480;
constexpr int L_KX = L_KVF + 4096, L_VX = L_KX + 8192, L_QF = L_KX + 16384;
constexpr int L_OX = L_KX + 4 * SEG_B;
constexpr int L_END = L_OX + 2 * 2304;
constexpr int L_WST = 0;
constexpr int CHAIN_LDS = 96 * 1024;

__device__ __forceinline__ bool spin_fail(unsigned& spins, gu32* ctl, bool& dead) {
  if (dead) return true;
  ++spins;
  if (spins > SPIN_CAP) { __hip_atomic_store(ctl + CTL_ABORT, 1u, RLX_AGENT); dead = true; return true; }
  if ((spins & 63u) == 0u && __builtin_amdgcn_readfirstlane(__hip_atomic_load(ctl + CTL_ABORT, RLX_AGENT))) { dead = true; return true; }
  return false;
}

#define LATE 24
#define PEN 100
#define CALREP 8
#define LATEW 3
#define WLD(p) __builtin_nontemporal_load(p)
#define PEN0 110
#define ESC 8
#define ATT 6
#define EPS1 16
#define EPS2 6
#define EPS3 18
#define SLIP 16
#define OXAUX 0
#define SAFE 4
#define PFLAG 4
#define D1_0 46
#define D2_0 91
#define D3_0 90

#define STAMP(k)
#define STAMPX(k) do { if (s >= 32 && s < 40) { SB(); unsigned c_ = clk32(); SB(); if (lane == 0) stamp[((s - 32) * 4 + w) * 16 + (k)] = c_; } } while (0)
#define STAMP2(k)
#define STAMP3(k)
#define STAMP4(k)

template <int CT, int K2> __device__ __forceinline__ void wo_def(v8u& d, const v8u& v);
template <int CT, int KK> __device__ __forceinline__ void wo_mf(f32x4& d, const v4u& a, const v8u (&wo8)[4][2]);
template <int CT, int K2> __device__ __forceinline__ void wo_smf(f32x4& d, const v4u& a, const v8u (&wo8)[4][2], int idx);
template <> __device__ __forceinline__ void wo_def<0, 0>(v8u& d, const v8u& v) { asm volatile("" : "={a[192:199]}"(d) : "0"(v)); }
template <> __device__ __forceinline__ void wo_smf<0, 0>(f32x4& d, const v4u& a, const v8u (&wo8)[4][2], int idx) {
  asm volatile("v_smfmac_f32_16x16x64_bf16 %0, %1, %2, %3" : "+v"(d) : "v"(a), "{a[192:199]}"(wo8[0][0]), "v"(idx));
}
template <> __device__ __forceinline__ void wo_mf<0, 0>(f32x4& d, const v4u& a, const v8u (&wo8)[4][2]) {
  asm volatile("v_mfma_f32_16x16x32_bf16 %0, %1, a[192:195], 0" : "=&v"(d) : "v"(a), "{a[192:199]}"(wo8[0][0]));
}
template <> __device__ __forceinline__ void wo_mf<0, 1>(f32x4& d, const v4u& a, const v8u (&wo8)[4][2]) {
  asm volatile("v_mfma_f32_16x16x32_bf16 %0, %1, a[196:199], %0" : "+v"(d) : "v"(a), "{a[192:199]}"(wo8[0][0]));
}
template <> __device__ __forceinline__ void wo_def<0, 1>(v8u& d, const v8u& v) { asm volatile("" : "={a[200:207]}"(d) : "0"(v)); }
template <> __device__ __forceinline__ void wo_smf<0, 1>(f32x4& d, const v4u& a, const v8u (&wo8)[4][2], int idx) {
  asm volatile("v_smfmac_f32_16x16x64_bf16 %0, %1, %2, %3" : "+v"(d) : "v"(a), "{a[200:207]}"(wo8[0][1]), "v"(idx));
}
template <> __device__ __forceinline__ void wo_mf<0, 2>(f32x4& d, const v4u& a, const v8u (&wo8)[4][2]) {
  asm volatile("v_mfma_f32_16x16x32_bf16 %0, %1, a[200:203], %0" : "+v"(d) : "v"(a), "{a[200:207]}"(wo8[0][1]));
}
template <> __device__ __forceinline__ void wo_mf<0, 3>(f32x4& d, const v4u& a, const v8u (&wo8)[4][2]) {
  asm volatile("v_mfma_f32_16x16x32_bf16 %0, %1, a[204:207], %0" : "+v"(d) : "v"(a), "{a[200:207]}"(wo8[0][1]));
}
template <> __device__ __forceinline__ void wo_def<1, 0>(v8u& d, const v8u& v) { asm volatile("" : "={a[208:215]}"(d) : "0"(v)); }
template <> __device__ __forceinline__ void wo_smf<1, 0>(f32x4& d, const v4u& a, const v8u (&wo8)[4][2], int idx) {
  asm volatile("v_smfmac_f32_16x16x64_bf16 %0, %1, %2, %3" : "+v"(d) : "v"(a), "{a[208:215]}"(wo8[1][0]), "v"(idx));
}
template <> __device__ __forceinline__ void wo_mf<1, 0>(f32x4& d, const v4u& a, const v8u (&wo8)[4][2]) {
  asm volatile("v_mfma_f32_16x16x32_bf16 %0, %1, a[208:211], 0" : "=&v"(d) : "v"(a), "{a[208:215]}"(wo8[1][0]));
}
template <> __device__ __forceinline__ void wo_mf<1, 1>(f32x4& d, const v4u& a, const v8u (&wo8)[4][2]) {
  asm volatile("v_mfma_f32_16x16x32_bf16 %0, %1, a[212:215], %0" : "+v"(d) : "v"(a), "{a[208:215]}"(wo8[1][0]));
}
template <> __device__ __forceinline__ void wo_def<1, 1>(v8u& d, const v8u& v) { asm volatile("" : "={a[216:223]}"(d) : "0"(v)); }
template <> __device__ __forceinline__ void wo_smf<1, 1>(f32x4& d, const v4u& a, const v8u (&wo8)[4][2], int idx) {
  asm volatile("v_smfmac_f32_16x16x64_bf16 %0, %1, %2, %3" : "+v"(d) : "v"(a), "{a[216:223]}"(wo8[1][1]), "v"(idx));
}
template <> __device__ __forceinline__ void wo_mf<1, 2>(f32x4& d, const v4u& a, const v8u (&wo8)[4][2]) {
  asm volatile("v_mfma_f32_16x16x32_bf16 %0, %1, a[216:219], %0" : "+v"(d) : "v"(a), "{a[216:223]}"(wo8[1][1]));
}
template <> __device__ __forceinline__ void wo_mf<1, 3>(f32x4& d, const v4u& a, const v8u (&wo8)[4][2]) {
  asm volatile("v_mfma_f32_16x16x32_bf16 %0, %1, a[220:223], %0" : "+v"(d) : "v"(a), "{a[216:223]}"(wo8[1][1]));
}
template <> __device__ __forceinline__ void wo_def<2, 0>(v8u& d, const v8u& v) { asm volatile("" : "={a[224:231]}"(d) : "0"(v)); }
template <> __device__ __forceinline__ void wo_smf<2, 0>(f32x4& d, const v4u& a, const v8u (&wo8)[4][2], int idx) {
  asm volatile("v_smfmac_f32_16x16x64_bf16 %0, %1, %2, %3" : "+v"(d) : "v"(a), "{a[224:231]}"(wo8[2][0]), "v"(idx));
}
template <> __device__ __forceinline__ void wo_mf<2, 0>(f32x4& d, const v4u& a, const v8u (&wo8)[4][2]) {
  asm volatile("v_mfma_f32_16x16x32_bf16 %0, %1, a[224:227], 0" : "=&v"(d) : "v"(a), "{a[224:231]}"(wo8[2][0]));
}
template <> __device__ __forceinline__ void wo_mf<2, 1>(f32x4& d, const v4u& a, const v8u (&wo8)[4][2]) {
  asm volatile("v_mfma_f32_16x16x32_bf16 %0, %1, a[228:231], %0" : "+v"(d) : "v"(a), "{a[224:231]}"(wo8[2][0]));
}
template <> __device__ __forceinline__ void wo_def<2, 1>(v8u& d, const v8u& v) { asm volatile("" : "={a[232:239]}"(d) : "0"(v)); }
template <> __device__ __forceinline__ void wo_smf<2, 1>(f32x4& d, const v4u& a, const v8u (&wo8)[4][2], int idx) {
  asm volatile("v_smfmac_f32_16x16x64_bf16 %0, %1, %2, %3" : "+v"(d) : "v"(a), "{a[232:239]}"(wo8[2][1]), "v"(idx));
}
template <> __device__ __forceinline__ void wo_mf<2, 2>(f32x4& d, const v4u& a, const v8u (&wo8)[4][2]) {
  asm volatile("v_mfma_f32_16x16x32_bf16 %0, %1, a[232:235], %0" : "+v"(d) : "v"(a), "{a[232:239]}"(wo8[2][1]));
}
template <> __device__ __forceinline__ void wo_mf<2, 3>(f32x4& d, const v4u& a, const v8u (&wo8)[4][2]) {
  asm volatile("v_mfma_f32_16x16x32_bf16 %0, %1, a[236:239], %0" : "+v"(d) : "v"(a), "{a[232:239]}"(wo8[2][1]));
}
template <> __device__ __forceinline__ void wo_def<3, 0>(v8u& d, const v8u& v) { asm volatile("" : "={a[240:247]}"(d) : "0"(v)); }
template <> __device__ __forceinline__ void wo_smf<3, 0>(f32x4& d, const v4u& a, const v8u (&wo8)[4][2], int idx) {
  asm volatile("v_smfmac_f32_16x16x64_bf16 %0, %1, %2, %3" : "+v"(d) : "v"(a), "{a[240:247]}"(wo8[3][0]), "v"(idx));
}
template <> __device__ __forceinline__ void wo_mf<3, 0>(f32x4& d, const v4u& a, const v8u (&wo8)[4][2]) {
  asm volatile("v_mfma_f32_16x16x32_bf16 %0, %1, a[240:243], 0" : "=&v"(d) : "v"(a), "{a[240:247]}"(wo8[3][0]));
}
template <> __device__ __forceinline__ void wo_mf<3, 1>(f32x4& d, const v4u& a, const v8u (&wo8)[4][2]) {
  asm volatile("v_mfma_f32_16x16x32_bf16 %0, %1, a[244:247], %0" : "+v"(d) : "v"(a), "{a[240:247]}"(wo8[3][0]));
}
template <> __device__ __forceinline__ void wo_def<3, 1>(v8u& d, const v8u& v) { asm volatile("" : "={a[248:255]}"(d) : "0"(v)); }
template <> __device__ __forceinline__ void wo_smf<3, 1>(f32x4& d, const v4u& a, const v8u (&wo8)[4][2], int idx) {
  asm volatile("v_smfmac_f32_16x16x64_bf16 %0, %1, %2, %3" : "+v"(d) : "v"(a), "{a[248:255]}"(wo8[3][1]), "v"(idx));
}
template <> __device__ __forceinline__ void wo_mf<3, 2>(f32x4& d, const v4u& a, const v8u (&wo8)[4][2]) {
  asm volatile("v_mfma_f32_16x16x32_bf16 %0, %1, a[248:251], %0" : "+v"(d) : "v"(a), "{a[248:255]}"(wo8[3][1]));
}
template <> __device__ __forceinline__ void wo_mf<3, 3>(f32x4& d, const v4u& a, const v8u (&wo8)[4][2]) {
  asm volatile("v_mfma_f32_16x16x32_bf16 %0, %1, a[252:255], %0" : "+v"(d) : "v"(a), "{a[248:255]}"(wo8[3][1]));
}

template <int W> __device__ __forceinline__ void chain_loop(const ChainArgs& a, LAS unsigned char* L, int cid, const v8u (&wqkv)[4][6], const v8u (&wo8)[4][2]);

__device__ __forceinline__ void chain_role(const ChainArgs& a, LAS unsigned char* L, int cid) {
  const int tid = threadIdx.x, lane = tid & 63, w = __builtin_amdgcn_readfirstlane(tid >> 6);
  const int h = (cid & 3) * 8 + (cid >> 4), g = (cid >> 2) & 3;
  const int l15 = lane & 15, lg = lane >> 4;
  if (cid == 0 && tid == 64) {
    unsigned lat[2] = {~0u, ~0u};
    for (int rep = 0; rep < CALREP; ++rep)
      for (int b = 0; b < 2; ++b) {
        const unsigned* p = (const unsigned*)(a.base + 65536 + b * 8192 + rep * 640);
        unsigned v;
        const unsigned t0 = clk32();
        asm volatile("global_load_dword %0, %1, off sc1\n\ts_waitcnt vmcnt(0)" : "=v"(v) : "v"(p) : "memory");
        lat[b] = min(lat[b], clk32() - t0);
      }
    __hip_atomic_store((gu32*)(a.base + CTL_OFF) + CTL_POL, lat[1] < lat[0] ? 1u : 0u, RLX_AGENT);
  }
  v8u wqkv[4][6];
  v8u wo8[4][2];
  {
    LAS unsigned char* st = L + L_WST + w * 16384;
    const int q4 = l15 >> 2, p4 = lane & 3;
    v4u ld[3][16];
    auto issue = [&](int m, v4u(&d)[16]) {
      if (m < 3) {
        const u16* W = (m == 0 ? a.Wq : m == 1 ? a.Wk : a.Wv) + (size_t)(256 * w + (lane >> 2)) * 4096 + h * 128 + 32 * g + 8 * (lane & 3);
#pragma unroll
        for (int i = 0; i < 16; ++i) d[i] = WLD((const GAS v4u*)(W + (size_t)i * 16 * 4096));
      } else {
        const u16* W = a.Wo + (size_t)(h * 128 + (lane >> 3)) * 1024 + 256 * g + 64 * w + 8 * (lane & 7);
#pragma unroll
        for (int i = 0; i < 16; ++i) d[i] = WLD((const GAS v4u*)(W + (size_t)i * 8 * 1024));
      }
      SB();
    };
    issue(0, ld[0]);
    issue(1, ld[1]);
    issue(2, ld[2]);
#pragma unroll
    for (int m = 0; m < 4; ++m) {
      v4u(&d)[16] = ld[m % 3];
      if (m < 3) {
#pragma unroll
        for (int i = 0; i < 16; ++i) {
          v2u ev = {__builtin_amdgcn_perm(d[i].y, d[i].x, 0x05040100u), __builtin_amdgcn_perm(d[i].w, d[i].z, 0x05040100u)};
          v2u od = {__builtin_amdgcn_perm(d[i].y, d[i].x, 0x07060302u), __builtin_amdgcn_perm(d[i].w, d[i].z, 0x07060302u)};
          LAS unsigned char* p = st + (16 * i + (lane >> 2)) * 64 + (lane & 3) * 8;
          *(LAS v2u*)p = ev;
          *(LAS v2u*)(p + 32) = od;
        }
      } else {
#pragma unroll
        for (int i = 0; i < 16; ++i) *(LAS v4u*)(st + i * 1024 + lane * 16) = d[i];
      }
      SB();
      if (m == 0) issue(3, d);
      asm volatile("s_waitcnt lgkmcnt(0)" ::: "memory");
      if (m < 3) {
        LAS unsigned char* rb = st + (16 * (lg >> 1) + 4 * (lg & 1) + (q4 & 1) + 8 * (q4 >> 1)) * 64 + p4 * 8;
#pragma unroll
        for (int kk = 0; kk < 4; ++kk)
#pragma unroll
          for (int T = 0; T < 2; ++T) {
            v2u r0 = trread(rb + (64 * kk) * 64 + T * 32), r1 = trread(rb + (64 * kk + 2) * 64 + T * 32);
            v2u r2 = trread(rb + (64 * kk + 32) * 64 + T * 32), r3 = trread(rb + (64 * kk + 34) * 64 + T * 32);
            v8u v = {r0.x, r0.y, r1.x, r1.y, r2.x, r2.y, r3.x, r3.y};
            asm volatile("" : "=a"(wqkv[kk][m * 2 + T]) : "0"(v));
          }
      } else {
        LAS unsigned char* rb = st + (8 * lg + q4) * 128 + p4 * 16;
        auto rd = [&](int ct, int k2) {
          LAS unsigned char* q = rb + 64 * k2 * 128 + (ct >> 1) * 64 + (ct & 1) * 8;
          v2u r0 = trread(q), r1 = trread(q + 4 * 128), r2 = trread(q + 32 * 128), r3 = trread(q + 36 * 128);
          return v8u{r0.x, r0.y, r1.x, r1.y, r2.x, r2.y, r3.x, r3.y};
        };
        wo_def<0, 0>(wo8[0][0], rd(0, 0)); wo_def<0, 1>(wo8[0][1], rd(0, 1)); wo_def<1, 0>(wo8[1][0], rd(1, 0)); wo_def<1, 1>(wo8[1][1], rd(1, 1));
        wo_def<2, 0>(wo8[2][0], rd(2, 0)); wo_def<2, 1>(wo8[2][1], rd(2, 1)); wo_def<3, 0>(wo8[3][0], rd(3, 0)); wo_def<3, 1>(wo8[3][1], rd(3, 1));
      }
      asm volatile("s_waitcnt lgkmcnt(0)" ::: "memory");
    }
  }
  if (w == 0) chain_loop<0>(a, L, cid, wqkv, wo8);
  else if (w == 1) chain_loop<1>(a, L, cid, wqkv, wo8);
  else if (w == 2) chain_loop<2>(a, L, cid, wqkv, wo8);
  else chain_loop<3>(a, L, cid, wqkv, wo8);
}

__device__ __forceinline__ float fmx(float a, float b) { return __builtin_elementwise_maximum(a, b); }
__device__ __forceinline__ float fadd(float a, float b) { float r; asm("v_add_f32 %0, %1, %2" : "=v"(r) : "v"(a), "v"(b)); return r; }
#define LD16(rs, vo_, imm, so) __builtin_amdgcn_raw_buffer_load_b128(rs, (vo_) + (imm), so, AUX_SC1)

__device__ __forceinline__ unsigned rtc32() {
  unsigned long long t;
  asm volatile("s_memrealtime %0\n\ts_waitcnt lgkmcnt(0)" : "=s"(t)::"memory");
  return (unsigned)t;
}
__device__ __forceinline__ bool wait_sched(unsigned T, unsigned extra) {
  unsigned now = rtc32();
  const bool late = (int)(now - T) > SLIP;
  if (late) T = now + LATE;
  T += extra;
  for (int i = 0; i < 4096 && (int)(now - T) < 0; ++i) now = rtc32();
  return late;
}
__device__ __forceinline__ unsigned wave_umax(unsigned v) {
  v = max(v, (unsigned)__builtin_amdgcn_update_dpp(0, (int)v, 0x111, 0xf, 0xf, true));
  v = max(v, (unsigned)__builtin_amdgcn_update_dpp(0, (int)v, 0x112, 0xf, 0xf, true));
  v = max(v, (unsigned)__builtin_amdgcn_update_dpp(0, (int)v, 0x114, 0xf, 0xf, true));
  v = max(v, (unsigned)__builtin_amdgcn_update_dpp(0, (int)v, 0x118, 0xf, 0xf, true));
  return max(max(__builtin_amdgcn_readlane(v, 15), __builtin_amdgcn_readlane(v, 31)), max(__builtin_amdgcn_readlane(v, 47), __builtin_amdgcn_readlane(v, 63)));
}
__device__ __forceinline__ int imax(int a, int b) { return a > b ? a : b; }
__device__ __forceinline__ int imin(int a, int b) { return a < b ? a : b; }

template <int W>
__device__ __forceinline__ void chain_loop(const ChainArgs& a, LAS unsigned char* L, int cid, const v8u (&wqkv)[4][6], const v8u (&wo8)[4][2]) {
  const int tid = threadIdx.x, lane = tid & 63;
  constexpr int w = W;
  const int h = (cid & 3) * 8 + (cid >> 4), g = (cid >> 2) & 3, cu = h * 4 + g;
  const int l15 = lane & 15, lg = lane >> 4;
  gu32* ctl = (gu32*)(a.base + CTL_OFF);
  bool dead = false;
  unsigned pol;
  for (unsigned spins = 0; (pol = __builtin_amdgcn_readfirstlane(__hip_atomic_load(ctl + CTL_POL, RLX_AGENT))) > 1u;) {
    __builtin_amdgcn_s_sleep(8);
    if (spin_fail(spins, ctl, dead)) break;
  }
  pol &= 1u;
  __amdgpu_buffer_rsrc_t rs = __builtin_amdgcn_make_buffer_rsrc(a.base, 0, (int)COMM_BYTES, 0x00020000);
  __amdgpu_buffer_rsrc_t rx = __builtin_amdgcn_make_buffer_rsrc(a.base + XQKV_OFF, 0, SEQ * LDX * 2, 0x00020000);
  __amdgpu_buffer_rsrc_t ro = __builtin_amdgcn_make_buffer_rsrc(a.base + OX_OFF, 0, SEQ * 4096 * 2, 0x00020000);
  const int so_w0 = (int)(pol << 13);
  auto so_part = [&](int slot) { return ((2 + slot) << 20) + (int)(((pol ^ 1u ^ (unsigned)(slot >> 1)) & 1u) << 13); };
  const int s1_half = (l15 >> 1) & 1;
  int a_mem = vo(w * 4096 + lane * 16);
  int sidx = s1_half ? 0xEEEE : 0x4444;
  const int pub_u = (w == 0 ? lg * 128 : 512 + lg * 256 + (w == 2 ? 128 : 0)) + l15 * 8;
  const int a_pub = vo(cu * 1536 + pub_u);
  int a_g2[3], l_g2[3], l_g2p[3];
#pragma unroll
  for (int i = 0; i < 3; ++i) {
    const int o = (w & 1) * 3072 + i * 1024 + lane * 16, kk = o / 1536, u = o % 1536;
    a_g2[i] = vo(h * 6144 + o);
    l_g2[i] = u < 512 ? (L_QF + kk * 1024 + (u >> 7) * 256 + (u & 127)) : (L_KVF + kk * 1024 + u - 512);
    l_g2p[i] = u < 512 ? SEG_B : 0;
  }
  const int m_tok = 2 * (l15 >> 2) + (l15 & 1);
  const int l_km = L_KVF + lg * 256 + m_tok * 16;
  const int l_vm = L_KVF + (lg >> 1) * 1024 + (lg & 1) * 512 + (8 + m_tok) * 16 + s1_half * 4;
  const unsigned l_red0 = (unsigned)(size_t)L + L_RED + w * 3456 + lg * 288 + l15 * 8, l_red1 = l_red0 + 1152;
  const int a_ps = vo((32 * g + 8 * w + 4 * (l15 >> 3) + 2 * (lg >> 1) + ((l15 >> 2) & 1)) * 4096 + h * 128 + (lg & 1) * 64 + (l15 & 3) * 16);
  int a_pg = vo(cu * 4096 + lane * 16);
  const int a_ms = (lane < 32 && l15 < 8) ? vo(cu * 128 + (4 * ((l15 >> 1) & 1) + 2 * (l15 >> 2) + (l15 & 1)) * 16 + lg * 8) : 0x40000000;
  v4u sel;
#pragma unroll
  for (int j = 0; j < 4; ++j) sel[j] = l15 == 2 * j ? 0x00003F80u : l15 == 2 * j + 1 ? 0x3F800000u : 0u;
  PIN1(sel);
  const int a_mr = lane < 8 ? vo(cu * 128 + lane * 16) : 0x40000000;
  const int a_qr0 = vo(cu * 1536 + lane * 16), a_qr1 = lane < 32 ? vo(cu * 1536 + 1024 + lane * 16) : 0x40000000;
  const int a_ts = lane == 0 ? vo(16384 + cu * 16) : 0x40000000;
  const int a_tg = vo(16384 + lane * 16);
  const int x_kv = (((w == 2 ? 32 : 64) + h) * SEQ + lg) * 256 + l15 * 16;
  const int x_q = (h * SEQ + 8 * g + 4 * (w & 1) + lg) * 256 + l15 * 16;
  const int l_q = L_QF + l15 * 256 + (8 + 4 * (w & 1) + lg) * 16;
  const int a_ox = (h * SEQ + 8 * g + lg) * 256 + l15 * 16;
  f32x4 msk[3];
#pragma unroll
  for (int r = 0; r < 4; ++r) {
    const int qi = 8 * g + l15 - 8;
    msk[0][r] = (l15 < 8 || 4 * lg + r <= qi) ? 0.f : -1e30f;
    msk[1][r] = (l15 < 8 || 16 + 4 * lg + r <= qi) ? 0.f : -1e30f;
    msk[2][r] = r < 2 ? 0.f : -1e30f;
  }
  v4u ones = {0x3F803F80u, 0x3F803F80u, 0x3F803F80u, 0x3F803F80u};
  v4u sentv = {SENT, SENT, SENT, SENT};
  v4u bm[4];
#pragma unroll
  for (int ct = 0; ct < 4; ++ct) bm[ct] = v4u{0u, 0u, 0u, 0u};
  PIN2(ones, sentv);
  PIN2(bm[0], bm[1]);
  PIN2(bm[2], bm[3]);
  PIN2(msk[0], msk[1]);
  PIN2(msk[2], sidx);
  LAS unsigned* sched = (LAS unsigned*)(L + L_SCHED);

  auto ox_store = [&](int s) {
#pragma unroll
    for (int j = 0; j < 2; ++j) {
      const v4u v = *(LAS v4u*)(L + L_OX + (s & 1) * 2304 + (4 * j + lg) * 272 + l15 * 16);
      __builtin_amdgcn_raw_buffer_store_b128(v, ro, a_ox, (s * 32 + 4 * j) * 256, AUX_SC1);
    }
    SB();
  };
#define ZTILE(ct) \
    wo_mf<ct, 0>(zc[(ct) & 1][0], vf[0][0], wo8); wo_mf<ct, 0>(zc[(ct) & 1][1], vf[1][0], wo8); \
    wo_mf<ct, 1>(zc[(ct) & 1][0], vf[0][1], wo8); wo_mf<ct, 1>(zc[(ct) & 1][1], vf[1][1], wo8); \
    wo_mf<ct, 2>(zc[(ct) & 1][0], vf[0][2], wo8); wo_mf<ct, 2>(zc[(ct) & 1][1], vf[1][2], wo8); \
    wo_mf<ct, 3>(zc[(ct) & 1][0], vf[0][3], wo8); wo_mf<ct, 3>(zc[(ct) & 1][1], vf[1][3], wo8)
#define ZCVT(ct) \
    PIN2(zc[(ct) & 1][0], zc[(ct) & 1][1]); \
    zx[ct] = v4u{cvt_pk(zc[(ct) & 1][0][0], zc[(ct) & 1][0][1]), cvt_pk(zc[(ct) & 1][0][2], zc[(ct) & 1][0][3]), \
                 cvt_pk(zc[(ct) & 1][1][0], zc[(ct) & 1][1][1]), cvt_pk(zc[(ct) & 1][1][2], zc[(ct) & 1][1][3])}; \
    PIN1(zx[ct]); SB()
  v4u zx[4];
  const int fo0 = lg * 256 + ((l15 ^ lg) * 16), fo1 = 1024 + lg * 256 + ((l15 ^ (4 + lg)) * 16);
  auto z_pair = [&](auto CT, int seg) {
    constexpr int ct = decltype(CT)::value;
    LAS unsigned char* vb = L + L_VX + (seg & 3) * SEG_B;
    v4u vf[2][4];
    f32x4 zc[2][2];
#pragma unroll
    for (int kk = 0; kk < 4; ++kk)
#pragma unroll
      for (int kt = 0; kt < 2; ++kt) vf[kt][kk] = *(LAS v4u*)(vb + ((kk & 1) ? fo1 : fo0) + kt * 4096 + (kk >> 1) * 2048);
    SB();
    ZTILE(ct);
    ZTILE(ct + 1);
    ZCVT(ct);
    NOPS(4);
    ZCVT(ct + 1);
  };
  v4u pf[9];
  auto pf_issue = [&](int s) {
    const int so = s * 32 * 256;
#pragma unroll
    for (int i = 0; i < 8; ++i) pf[i] = __builtin_amdgcn_raw_buffer_load_b128(rx, x_kv, so + i * 1024, AUX_SC1);
    pf[8] = __builtin_amdgcn_raw_buffer_load_b128(rx, x_q, so, AUX_SC1);
    SB();
  };
  auto pf_write = [&](int s) {
    LAS unsigned char* d = L + (w == 2 ? L_KX : L_VX) + (s & 3) * SEG_B + l15 * 256;
#pragma unroll
    for (int i = 0; i < 8; ++i) {
      const int row = 4 * i + lg;
      *(LAS v4u*)(d + (row >> 4) * 4096 + (((row & 15) ^ (l15 & 7)) * 16)) = pf[i];
    }
    *(LAS v4u*)(L + l_q + (s & 3) * SEG_B) = pf[8];
  };
  BAR();
  auto rb_wait = [&](unsigned have, int rb) {
    for (unsigned spins = 0; have < 96u; have = __builtin_amdgcn_readfirstlane(__hip_atomic_load(ctl + CTL_RB + 64 * rb, RLX_AGENT))) {
      __builtin_amdgcn_s_sleep(8);
      if (spin_fail(spins, ctl, dead)) break;
    }
    __builtin_amdgcn_fence(__ATOMIC_ACQUIRE, "wavefront");
  };
  unsigned rbf = 96u;
  if (w >= 2) { rb_wait(0u, 0); pf_issue(0); pf_write(0); pf_issue(1); }
  const unsigned tb = rtc32() - (1u << 24);
  int esc = 0, c1 = 0, p1 = 0, p2 = 0, p3 = 0, d1 = D1_0, d2 = D2_0, d3 = D3_0, m1 = D1_0, m2 = D2_0, m3 = D3_0;
  bool pok = false;
  unsigned I1 = tb, I2 = tb, I3 = tb, I2n = tb, I3n = tb;
  if (w == 3 && lane == 0) { *(LAS v4u*)sched = v4u{0u, 0u, 0u, ~0u}; sched[4] = 0u; sched[5] = 0u; }
  BAR();
  if (w == 0) {
    const unsigned t = min(rtc32(), SENT - 1u);
    __builtin_amdgcn_raw_buffer_store_b128(v4u{t, t, t, 0u}, rs, a_ts, so_w0 + 3 * MEM_SLOT, AUX_SC1);
  }
  z_pair(std::integral_constant<int, 0>(), 0);
  if (w >= 2) z_pair(std::integral_constant<int, 2>(), 0);

#define CNT(k)
  for (int s = 0; s < NSEG; ++s) {
    const int sb = (s & 3) * SEG_B;
    STAMP(0);
    v4u kxf[2][4], mf[4];
    const int so_m = so_w0 + ((s + 3) & 3) * MEM_SLOT;
    if (w != 0) {
      for (int i = 0; i < (1 << 24); ++i) {
        unsigned f;
        asm volatile("ds_read_b32 %0, %1\n\ts_waitcnt lgkmcnt(0)" : "=v"(f) : "v"((unsigned)(size_t)(sched + 4)) : "memory");
        if (__builtin_amdgcn_readfirstlane(f) == (unsigned)s) break;
      }
    }
    bool off = wait_sched(I1, 0);
    STAMP4(0);
    PIN1(a_mem);
#pragma unroll
    for (int kk = 0; kk < 4; ++kk) mf[kk] = LD16(rs, a_mem, kk * 1024, so_m);
    SB();
    if (w >= 2) {
      pf_write(s + 1);
      rb_wait(__builtin_amdgcn_readfirstlane(rbf), min(s + 2, NSEG - 1) >> 2);
    }
    if (w < 2) z_pair(std::integral_constant<int, 2>(), s);
    f32x4 acc[6];
#pragma unroll
    for (int j = 0; j < 6; ++j) { acc[j] = f32x4{0.f, 0.f, 0.f, 0.f}; PIN1(acc[j]); }
    NOPS(2);
    SB();
    STAMP(1);
    STAMP4(1);
    bool bad = false;
    {
      unsigned mx = 0;
#pragma unroll
      for (int kk = 0; kk < 4; ++kk) {
        SMF_A(acc[0], mf[kk], wqkv[kk][0], sidx);
        mx = umax4(mx, mf[kk]);
        PIN1(mx);
        SB();
        SMF_A(acc[1], mf[kk], wqkv[kk][1], sidx);
        if (kk == 3) { bad = __any(mx == SENT); SB(); }
#pragma unroll
        for (int j = 2; j < 6; ++j) SMF_A(acc[j], mf[kk], wqkv[kk][j], sidx);
        SB();
        if (kk == 0) STAMP4(2);
      }
    }
    if (__builtin_expect(bad, 0)) {
      v4u m2f[4] = {mf[0], mf[1], mf[2], mf[3]};
      for (unsigned spins = 0;;) {
        CNT(0);
        if (spin_fail(spins, ctl, dead)) break;
        bool b[4];
#pragma unroll
        for (int kk = 0; kk < 4; ++kk) b[kk] = __any(umax4(0u, m2f[kk]) == SENT);
        if (!(b[0] | b[1] | b[2] | b[3])) break;
        PIN1(a_mem);
#pragma unroll
        for (int kk = 0; kk < 4; ++kk)
          if (b[kk]) m2f[kk] = LD16(rs, a_mem, kk * 1024, so_m);
      }
      off = true;
      f32x4 r[6];
#pragma unroll
      for (int j = 0; j < 6; ++j) { r[j] = f32x4{0.f, 0.f, 0.f, 0.f}; PIN1(r[j]); }
      NOPS(2);
      SB();
#pragma unroll
      for (int kk = 0; kk < 4; ++kk)
#pragma unroll
        for (int j = 0; j < 6; ++j) SMF_A(r[j], m2f[kk], wqkv[kk][j], sidx);
      NOPS(8);
#pragma unroll
      for (int j = 0; j < 6; ++j) { PIN1(r[j]); acc[j] = r[j]; }
      SB();
    }
    STAMP(2);
    STAMP4(3);
    {
      NOPS(3);
#pragma unroll
      for (int m = 0; m < 3; ++m) {
        f32x2 u, v;
        asm volatile("v_add_f32 %0, %4, %5\n\tv_add_f32 %1, %6, %7\n\tv_add_f32 %2, %8, %9\n\tv_add_f32 %3, %10, %11"
                     : "=&v"(u.x), "=&v"(u.y), "=&v"(v.x), "=&v"(v.y)
                     : "v"(acc[2 * m][0]), "v"(acc[2 * m][2]), "v"(acc[2 * m + 1][0]), "v"(acc[2 * m + 1][2]), "v"(acc[2 * m][1]), "v"(acc[2 * m][3]),
                       "v"(acc[2 * m + 1][1]), "v"(acc[2 * m + 1][3]));
        asm volatile("ds_write2_b64 %0, %1, %2 offset0:%3 offset1:%4" ::"v"(m < 1 ? l_red0 : l_red1), "v"(u), "v"(v), "i"(m == 2 ? 144 : 0), "i"(m == 2 ? 162 : 18)
                     : "memory");
      }
    }
    if (__builtin_expect(off, 0)) sched[5] = (unsigned)(s + 1);
    STAMP4(4);
    BAR();
    v4u tsv[2];
    STAMP4(5);
    const int so_q = so_w0 + QKV_BASE + (s & 3) * QKV_SLOT;
    unsigned offw = 0;
    bool offs = false;
    if (w < 3) {
      LAS f32x4* rr = (LAS f32x4*)(L + L_RED + w * 1152 + (l15 >> 1) * 144 + lg * 32 + (lane & 1) * 16);
      f32x4 x0 = rr[0], x1 = rr[216], x2 = rr[432], x3 = rr[648];
      offw = sched[5];
      f32x4 x = (x0 + x1) + (x2 + x3);
      if (w == 0) x *= CEXP;
      __builtin_amdgcn_raw_buffer_store_b64(v2u{cvt_pk(x[0], x[1]), cvt_pk(x[2], x[3])}, rs, a_pub, so_q, AUX_SC1);
      SB();
    }
    STAMP(3);
    STAMP4(6);
    unsigned tq = 0;
    v4u q2[3];
    int lw[3];
    if (w == 0) tq = rtc32();
    auto plan = [&]() {
      int a1 = 0, n2 = 0, n3 = 0, n1 = 0;
      bool ok = false;
      {
        for (unsigned spins = 0;;) {
          if (!__any(umax3(umax3(tsv[0].x, tsv[0].y, tsv[0].z), tsv[1].x, umax3(0u, tsv[1].y, tsv[1].z)) == SENT)) break;
          if (spin_fail(spins, ctl, dead)) break;
          tsv[0] = LD16(rs, a_tg, 0, so_m);
          tsv[1] = LD16(rs, a_tg, 0, so_m + 1024);
          PIN2(tsv[0], tsv[1]);
        }
        const int Tm = (int)wave_umax(max(tsv[0].z - tb, tsv[1].z - tb));
        ok = Tm + EPS1 - SAFE <= c1;
        esc = ok ? 0 : imin(esc + ESC, 8 * ESC);
        a1 = ok ? c1 : imax(c1, Tm) + (s ? PEN : PEN0);
        n2 = ok ? (int)(I2n - tb) : a1 + d2 + LATE;
        n3 = ok ? (int)(I3n - tb) : n2 + d3 + EPS3 + esc - ESC;
        n1 = n3 + d1 + EPS1 + (esc >> 1);
        I2 = tb + (unsigned)n2; I3 = tb + (unsigned)n3; I1 = tb + (unsigned)n1;
        const int Tq = (int)wave_umax(max(tsv[0].x - tb, tsv[1].x - tb)), Tp = (int)wave_umax(max(tsv[0].y - tb, tsv[1].y - tb));
        if (pok) { const int m = Tq - p1; d2 = imin(imax(m, m2), d2 + ATT); m2 = m; }
        if (pok && Tq + EPS2 - SAFE <= p2) { const int m = Tp - p2; d3 = imin(imax(m, m3), d3 + ATT); m3 = m; }
        if (s && Tp + EPS3 - SAFE <= p3) { const int m = Tm - p3; d1 = imin(imax(m, m1), d1 + ATT); m1 = m; }
      }
      I2n = I1 + (unsigned)(d2 + EPS2);
      I3n = I2n + (unsigned)(d3 + EPS3);
      if (lane == 0) {
        *(LAS v2u*)(sched + 6) = v2u{I2n, I3n};
        *(LAS v4u*)sched = v4u{I1, I2, I3, (unsigned)s};
      }
      p1 = a1; p2 = n2; p3 = n3; c1 = n1; pok = ok;
    };
    if (w == 3) {
      offw = sched[5];
      asm volatile("s_waitcnt vmcnt(0)" ::: "memory");
    }
    auto get_sched = [&]() {
      for (;;) {
        v4u sc;
        v2u nx;
        asm volatile("ds_read_b128 %0, %2\n\tds_read_b64 %1, %2 offset:24\n\ts_waitcnt lgkmcnt(0)" : "=&v"(sc), "=&v"(nx) : "v"((unsigned)(size_t)sched) : "memory");
        if (__builtin_amdgcn_readfirstlane(sc.w) == (unsigned)s) {
          I1 = __builtin_amdgcn_readfirstlane(sc.x);
          I2 = __builtin_amdgcn_readfirstlane(sc.y);
          I3 = __builtin_amdgcn_readfirstlane(sc.z);
          I2n = __builtin_amdgcn_readfirstlane(nx.x);
          I3n = __builtin_amdgcn_readfirstlane(nx.y);
          break;
        }
      }
    };
    auto k_frags = [&]() {
#pragma unroll
      for (int kt = 0; kt < 2; ++kt)
#pragma unroll
        for (int kk = 0; kk < 4; ++kk) kxf[kt][kk] = *(LAS v4u*)(L + L_KX + sb + ((kk & 1) ? fo1 : fo0) + kt * 4096 + (kk >> 1) * 2048);
      SB();
    };
    if (w < 2) {
#pragma unroll
      for (int i = 0; i < 3; ++i) { lw[i] = l_g2[i] + (s & 3) * l_g2p[i]; PIN1(lw[i]); }
      if (w == 1) {
        tsv[0] = LD16(rs, a_tg, 0, so_m);
        tsv[1] = LD16(rs, a_tg, 0, so_m + 1024);
        SB();
      }
      offs = __builtin_amdgcn_readfirstlane(offw) == (unsigned)(s + 1);
      I3 = I3n;
      wait_sched(offs ? tb : I2n, 0);
      STAMP2(0);
#pragma unroll
      for (int i = 0; i < 3; ++i) q2[i] = LD16(rs, a_g2[i], 0, so_q);
      SB();
      k_frags();
    } else {
      k_frags();
      offs = __builtin_amdgcn_readfirstlane(offw) == (unsigned)(s + 1);
      const unsigned Tpf = offs ? rtc32() + LATE + (w == 3 ? 12 : 2) : I2n + PFLAG;
      for (int i = 0; i < 4096 && (int)(rtc32() - Tpf) < 0; ++i) {}
      pf_issue(min(s + 2, NSEG - 1));
      if (w == 3 && s > 0) {
        if ((s & 7) == 1 && s > 1 && s <= 8 * NRB1 + 1 && lane == 0)
          __hip_atomic_fetch_add(ctl + CTL_OX + 64 * (((s >> 3) - 1) * 4 + (h >> 3)), 1u, RLX_AGENT);
        ox_store(s - 1);
      }
      if (w == 3) {
        PIN1(a_pg);
#pragma unroll
        for (int k = 0; k < 4; ++k) __builtin_amdgcn_raw_buffer_store_b128(sentv, rs, a_pg + k * 1024, so_part((s + 3) & 3), AUX_SC1);
      }
      rbf = __hip_atomic_load(ctl + CTL_RB + 64 * (min(s + 3, NSEG - 1) >> 2), RLX_AGENT);
      if (w == 2) {
        const int so_r = so_w0 + QKV_BASE + ((s + 3) & 3) * QKV_SLOT;
        __builtin_amdgcn_raw_buffer_store_b128(sentv, rs, a_qr0, so_r, AUX_SC1);
        __builtin_amdgcn_raw_buffer_store_b128(sentv, rs, a_qr1, so_r, AUX_SC1);
        __builtin_amdgcn_raw_buffer_store_b128(sentv, rs, a_mr, so_w0 + ((s + 2) & 3) * MEM_SLOT, AUX_SC1);
        __builtin_amdgcn_raw_buffer_store_b128(sentv, rs, a_ts, so_w0 + ((s + 1) & 3) * MEM_SLOT, AUX_SC1);
      }
      SB();
    }
    f32x4 vw[4];
#pragma unroll
    for (int ct = 0; ct < 4; ++ct) { vw[ct] = f32x4{0.f, 0.f, 0.f, 0.f}; PIN1(vw[ct]); }
    SB();
    if (w < 2) {
      for (unsigned spins = 0;;) {
        unsigned mx = 0;
#pragma unroll
        for (int i = 0; i < 3; ++i) {
          *(LAS v4u*)(L + lw[i]) = q2[i];
          mx = umax4(mx, q2[i]);
          SB();
          if (i == 0) STAMP2(1);
        }
        if (!__any(mx == SENT)) break;
        CNT(1);
        if (spin_fail(spins, ctl, dead)) break;
#pragma unroll
        for (int i = 0; i < 3; ++i) q2[i] = LD16(rs, a_g2[i], 0, so_q);
        SB();
      }
    }
    STAMP(4);
    STAMP2(2);
    BAR();
    STAMP2(3);
    v4u bx;
    float rinv;
    const int so_p = so_part(s & 3);
    {
      v4u qf[4], kmf[4], vms[2];
#pragma unroll
      for (int kk = 0; kk < 4; ++kk) {
        qf[kk] = *(LAS v4u*)(L + L_QF + sb + kk * 1024 + lane * 16);
        SB();
        kmf[kk] = *(LAS v4u*)(L + l_km + kk * 1024);
        SB();
      }
#pragma unroll
      for (int k2 = 0; k2 < 2; ++k2) {
        LAS unsigned* vp = (LAS unsigned*)(L + l_vm + k2 * 2048);
        vms[k2] = v4u{vp[0], vp[2], vp[64], vp[66]};
      }
      SB();
      f32x4 s0, s1, sm;
      MF_C(s0, kxf[0][0], qf[0], msk[0]);
      MF_C(s1, kxf[1][0], qf[0], msk[1]);
      MF_C(sm, kmf[0], qf[0], msk[2]);
#pragma unroll
      for (int kk = 1; kk < 4; ++kk) {
        MF_A(s0, kxf[0][kk], qf[kk], "v");
        MF_A(s1, kxf[1][kk], qf[kk], "v");
        MF_A(sm, kmf[kk], qf[kk], "v");
      }
#define VW(i) wo_smf<(i) & 3, ((i) >> 2)>(vw[(i) & 3], vms[(i) >> 2], wo8, sidx)
#define EX(x) __builtin_amdgcn_exp2f(x)
      STAMP2(4);
      float e0[4], e1[4], em[2];
      VW(0); VW(1);
      NOPS(4);
      PIN1(s0);
      e0[0] = EX(s0[0]);
      PIN1(e0[0]);
      SB();
      VW(2);
      e0[1] = EX(s0[1]);
      SB();
      VW(3);
      e0[2] = EX(s0[2]);
      e0[3] = EX(s0[3]);
      SB();
      VW(4);
      PIN1(s1);
      e1[0] = EX(s1[0]);
      bx.x = cvt_pk(e0[0], e0[1]);
      SB();
      VW(5);
      e1[1] = EX(s1[1]);
      bx.y = cvt_pk(e0[2], e0[3]);
      SB();
      VW(6);
      e1[2] = EX(s1[2]);
      e1[3] = EX(s1[3]);
      SB();
      VW(7);
      PIN1(sm);
      em[0] = EX(sm[0]);
      bx.z = cvt_pk(e1[0], e1[1]);
      bx.w = cvt_pk(e1[2], e1[3]);
      PIN1(bx);
      SB();
      em[1] = EX(sm[1]);
      PIN2(em[0], em[1]);
      NOPS(1);
      SB();
      STAMP2(6);
      f32x4 ls, o[4];
      v4u zm;
      MF_Z(ls, ones, bx, "v");
      bm[0].x = cvt_pk(em[0], em[1]);
      PIN2(vw[0], bm[0]);
      float f0 = fadd(vw[0][0], vw[0][2]), f1 = fadd(vw[0][1], vw[0][3]);
      SB();
      MF_Z(o[0], zx[0], bx, "v");
      PIN1(vw[1]);
      float f2 = fadd(vw[1][0], vw[1][2]), f3 = fadd(vw[1][1], vw[1][3]);
      zm.x = cvt_pk(f0, f1);
      SB();
      MF_A(ls, ones, bm[0], "v");
      PIN1(vw[2]);
      f0 = fadd(vw[2][0], vw[2][2]); f1 = fadd(vw[2][1], vw[2][3]);
      zm.y = cvt_pk(f2, f3);
      SB();
      MF_Z(o[1], zx[1], bx, "v");
      PIN1(vw[3]);
      f2 = fadd(vw[3][0], vw[3][2]); f3 = fadd(vw[3][1], vw[3][3]);
      zm.z = cvt_pk(f0, f1);
      SB();
      MF_Z(o[2], zx[2], bx, "v");
      zm.w = cvt_pk(f2, f3);
      bm[1].y = bm[0].x;
      PIN2(zm, bm[1]);
      SB();
      MF_Z(o[3], zx[3], bx, "v");
      bm[2].z = bm[0].x;
      bm[3].w = bm[0].x;
      PIN2(bm[2], bm[3]);
      SB();
      MF_A(o[0], zm, bm[0], "v");
      MF_A(o[1], zm, bm[1], "v");
      PIN1(ls);
      rinv = __builtin_amdgcn_rcpf(ls[0]);
      const bool redo = __any(!(ls[0] > 1e-15f && ls[0] < 1e15f));
      PIN1(rinv);
      SB();
      MF_A(o[2], zm, bm[2], "v");
      MF_A(o[3], zm, bm[3], "v");
      if (__builtin_expect(redo || FORCE_SAFE, 0)) {
        float mx = fmx(fmx(fmx(fmx(s0[0], s0[1]), fmx(s0[2], s0[3])), fmx(fmx(s1[0], s1[1]), fmx(s1[2], s1[3]))), fmx(sm[0], sm[1]));
        auto r = __builtin_amdgcn_permlane16_swap(ubits(mx), ubits(mx), false, false);
        mx = fmx(fbits(r[0]), fbits(r[1]));
        auto r2 = __builtin_amdgcn_permlane32_swap(ubits(mx), ubits(mx), false, false);
        mx = fmx(fbits(r2[0]), fbits(r2[1]));
        v4u cx = {cvt_pk(EX(s0[0] - mx), EX(s0[1] - mx)), cvt_pk(EX(s0[2] - mx), EX(s0[3] - mx)), cvt_pk(EX(s1[0] - mx), EX(s1[1] - mx)),
                  cvt_pk(EX(s1[2] - mx), EX(s1[3] - mx))};
        const unsigned pm = cvt_pk(EX(sm[0] - mx), EX(sm[1] - mx));
        v4u cm[4] = {v4u{pm, 0u, 0u, 0u}, v4u{0u, pm, 0u, 0u}, v4u{0u, 0u, pm, 0u}, v4u{0u, 0u, 0u, pm}};
        PIN2(cm[0], cm[1]);
        PIN2(cm[2], cm[3]);
        PIN1(cx);
        NOPS(8);
        f32x4 l2, o2[4];
        MF_Z(l2, ones, cx, "v");
        MF_A(l2, ones, cm[0], "v");
#pragma unroll
        for (int ct = 0; ct < 4; ++ct) { MF_Z(o2[ct], zx[ct], cx, "v"); MF_A(o2[ct], zm, cm[ct], "v"); }
        NOPS(8);
        PIN2(o2[0], o2[1]);
        PIN2(o2[2], o2[3]);
        PIN1(l2);
        rinv = __builtin_amdgcn_rcpf(l2[0]);
        bx = cx;
        bm[0].x = pm;
#pragma unroll
        for (int ct = 0; ct < 4; ++ct) o[ct] = o2[ct];
        PIN2(o[0], o[1]);
        PIN2(o[2], o[3]);
        NOPS(2);
      }
      NOPS(2);
      PIN2(o[0], o[1]);
      v4u va = {cvt_pk(o[0][0] * rinv, o[0][1] * rinv), cvt_pk(o[0][2] * rinv, o[0][3] * rinv), cvt_pk(o[1][0] * rinv, o[1][1] * rinv), cvt_pk(o[1][2] * rinv, o[1][3] * rinv)};
      PIN1(va);
      SB();
      PIN2(o[2], o[3]);
      v4u vb = {cvt_pk(o[2][0] * rinv, o[2][1] * rinv), cvt_pk(o[2][2] * rinv, o[2][3] * rinv), cvt_pk(o[3][0] * rinv, o[3][1] * rinv), cvt_pk(o[3][2] * rinv, o[3][3] * rinv)};
#pragma unroll
      for (int r = 0; r < 4; ++r) va[r] = __builtin_amdgcn_update_dpp(va[r], vb[r], 0x118, 0xf, 0xc, false);
      __builtin_amdgcn_raw_buffer_store_b128(va, rs, a_ps, so_p, AUX_SC1);
      SB();
    }
    STAMP(5);
    STAMP2(7);
    v4u pg[4];
    unsigned tp = 0;
    if (w == 0) tp = rtc32();
    if (w == 1) plan();
    auto ox_part = [&]() {
      const int q4 = l15 >> 2, p4 = lane & 3;
      f32x4 o[2];
      v4u ta[2], tb2[2];
#pragma unroll
      for (int j = 0; j < 2; ++j) {
        const int chunk = 2 * (2 * w + j) + (p4 >> 1);
        LAS unsigned char* vp = L + L_VX + sb + chunk * 256 + (((4 * lg + q4) ^ (chunk & 7)) * 16) + (p4 & 1) * 8;
        v2u t0 = trread(vp), t1 = trread(vp + 4096);
        v2u tm = trread(L + L_KVF + chunk * 256 + (8 + 2 * lg + (q4 & 1)) * 16 + (p4 & 1) * 8);
        ta[j] = v4u{t0.x, t0.y, t1.x, t1.y};
        tb2[j] = v4u{tm.x, tm.y, t0.x, t0.y};
      }
      PIN2(ta[0], tb2[0]);
      PIN2(ta[1], tb2[1]);
      NOPS(2);
      MF_Z(o[0], ta[0], bx, "v");
      MF_Z(o[1], ta[1], bx, "v");
      MF_A(o[0], tb2[0], bm[0], "v");
      MF_A(o[1], tb2[1], bm[0], "v");
      NOPS(8);
      PIN2(o[0], o[1]);
#pragma unroll
      for (int j = 0; j < 2; ++j)
        *(LAS v2u*)(L + L_OX + (s & 1) * 2304 + (l15 >= 8 ? (l15 - 8) * 272 + (32 * w + 16 * j + 4 * lg) * 2 : 2176 + lg * 8)) =
            v2u{cvt_pk(o[j][0] * rinv, o[j][1] * rinv), cvt_pk(o[j][2] * rinv, o[j][3] * rinv)};
      SB();
    };
    ox_part();
    if (w == 0) {
      if (offs) get_sched();
      wait_sched(I3, 0);
      STAMP3(0);
      PIN1(a_pg);
#pragma unroll
      for (int k = 0; k < 4; ++k) pg[k] = LD16(rs, a_pg, k * 1024, so_p);
      SB();
    }
    z_pair(std::integral_constant<int, 0>(), s + 1);
    if (w >= 2) z_pair(std::integral_constant<int, 2>(), s + 1);
    if (w >= 2) get_sched();
    if (w == 0 && s + 1 < NSEG) {
      STAMP3(1);
      f32x4 sum;
      for (unsigned spins = 0;;) {
        unsigned mx = umax4(0u, pg[0]);
        MF_Z(sum, sel, pg[0], "v");
#pragma unroll
        for (int k = 1; k < 4; ++k) {
          MF_A(sum, sel, pg[k], "v");
          mx = umax4(mx, pg[k]);
          PIN1(mx);
          SB();
          if (k == 1) STAMP3(2);
        }
        STAMP3(3);
        if (!__any(mx == SENT)) break;
        CNT(2);
        if (spin_fail(spins, ctl, dead)) break;
        PIN1(a_pg);
#pragma unroll
        for (int k = 0; k < 4; ++k) pg[k] = LD16(rs, a_pg, k * 1024, so_p);
        NOPS(4);
        SB();
      }
      const unsigned tm = rtc32();
      v4u tsw = {min(tq, SENT - 1u), min(tp, SENT - 1u), min(tm, SENT - 1u), 0u};
      PIN2(sum, tsw);
#pragma unroll
      for (int r = 0; r < 4; ++r) sum[r] += fbits(__builtin_amdgcn_update_dpp(0u, ubits(sum[r]), 0x128, 0xf, 0xf, true));
      const int so_o = so_w0 + (s & 3) * MEM_SLOT;
      __builtin_amdgcn_raw_buffer_store_b64(v2u{cvt_pk(sum[0], sum[1]), cvt_pk(sum[2], sum[3])}, rs, a_ms, so_o, AUX_SC1);
      SB();
      __builtin_amdgcn_raw_buffer_store_b128(tsw, rs, a_ts, so_o, AUX_SC1);
      sched[4] = (unsigned)(s + 1);
      SB();
      get_sched();
      SB();
    }
    STAMP(6);
    STAMP3(4);
  }
  BAR();
  if (w == 3) ox_store(NSEG - 1);
}

#define DMAPOL ""
#define AP1 1
#define AP2 3
#define AP3 4
#define AP4 5
#define AP5 7
#define ABL 0
#define GDMA0 1
#define GDMAS 4
#define GBAR 28
#define GSTAMP(j)
#define SSTAMP()
struct GItem {
  const u16* A; const u16* W; u16* C;
  int lda, ldw, ldc, nk;
  int ajump;
  int cjump;
  float scale;
  int flag;
  int two;
};
constexpr int G_STAGE = 131072;
constexpr int GEMM_LDS = G_STAGE + 16384;

__device__ __forceinline__ f32x16 mfma32(v4u a, v4u b, f32x16 c) {
  return __builtin_amdgcn_mfma_f32_32x32x16_bf16(__builtin_bit_cast(bf16x8, a), __builtin_bit_cast(bf16x8, b), c, 0, 0, 0);
}
__device__ __forceinline__ void dma16(unsigned m0v, int voff, __amdgpu_buffer_rsrc_t rs, int soff) {
  asm volatile("s_mov_b32 m0, %0\n\ts_nop 0\n\tbuffer_load_dwordx4 %1, %2, %3 offen " DMAPOL " lds" ::"s"(m0v), "v"(voff), "s"(rs), "s"(soff) : "memory");
}
__device__ __forceinline__ __amdgpu_buffer_rsrc_t mk_rsrc(const void* p, int bytes) {
  const unsigned long long u = (unsigned long long)p;
  const unsigned lo = __builtin_amdgcn_readfirstlane((unsigned)u), hi = __builtin_amdgcn_readfirstlane((unsigned)(u >> 32));
  return __builtin_amdgcn_make_buffer_rsrc((void*)(((unsigned long long)hi << 32) | lo), 0, __builtin_amdgcn_readfirstlane(bytes), 0x00020000);
}

struct GStream {
  __amdgpu_buffer_rsrc_t ra0, ra1, rw;
  int va0[2], va1[2], vw[2];
  int sa0, sa1, sw;
  int ra0s, ra1s, rws;
  int ja0, ja1;
};
struct GCtx {
  LAS unsigned char* L;
  unsigned ldsb;
  int lane, w;
  int aoff[2], boff[4];
};
__device__ __forceinline__ void gs_set_a0(GStream& g, const GItem& it, bool valid, int lane, int w) {
  g.ra0 = mk_rsrc(it.A, valid ? (255 * it.lda + (it.nk / 2 - 1) * (64 + it.ajump) + 128) * 2 : 0);
#pragma unroll
  for (int e = 0; e < 2; ++e) g.va0[e] = (32 * w + 8 * e + (lane >> 3)) * it.lda * 2 + (((lane & 7) ^ (4 * e + (lane >> 4))) * 16);
  g.sa0 = 0;
  g.ra0s = 32 * it.lda;
  g.ja0 = it.ajump * 2;
}
__device__ __forceinline__ void gs_set_rest(GStream& g, const GItem& it, bool valid, int lane, int w) {
  g.ra1 = mk_rsrc(it.A, valid ? (255 * it.lda + (it.nk / 2 - 1) * (64 + it.ajump) + 128) * 2 : 0);
#pragma unroll
  for (int e = 0; e < 2; ++e) g.va1[e] = (128 + 32 * w + 8 * e + (lane >> 3)) * it.lda * 2 + (((lane & 7) ^ (4 * e + (lane >> 4))) * 16);
  g.sa1 = 0;
  g.ra1s = 32 * it.lda;
  g.ja1 = it.ajump * 2;
  g.rw = mk_rsrc(it.W, valid ? ((it.nk * 64 - 1) * it.ldw + 256) * 2 : 0);
#pragma unroll
  for (int e = 0; e < 2; ++e) g.vw[e] = (16 * w + (lane >> 4)) * it.ldw * 2 + (((lane & 15) ^ ((lane >> 4) << 2) ^ (e << 1)) * 16);
  g.sw = 0;
  g.rws = 8 * it.ldw;
}
template <int KIND, int P>
__device__ __forceinline__ void g_dma(const GCtx& c, const GStream& g, int i) {
  constexpr unsigned slot = KIND < 2 ? (P * 2 + KIND) * 16384u : 65536u + (P * 2 + KIND - 2) * 16384u;
  const unsigned m0v = c.ldsb + slot + i * 1024u;
  if (KIND == 0) dma16(m0v, g.va0[i & 1], g.ra0, g.sa0 + (i >> 1) * g.ra0s);
  else if (KIND == 1) dma16(m0v, g.va1[i & 1], g.ra1, g.sa1 + (i >> 1) * g.ra1s);
  else dma16(m0v, g.vw[i >> 1], g.rw, g.sw + i * g.rws + (KIND - 2) * 256);
}
template <int H, int P>
__device__ __forceinline__ v4u g_rda(const GCtx& c, int mt, int ks) {
  return *(LAS v4u*)(c.L + c.aoff[ks] + (P * 2 + H) * 16384 + mt * 2048);
}
template <int H, int P>
__device__ __forceinline__ v4u g_rdb(const GCtx& c, int nt, int ks) {
  LAS unsigned char* p = c.L + c.boff[nt] + (P * 2 + H) * 16384 + ks * 8192;
  const v2u lo = trread(p), hi = trread(p + 1024);
  return v4u{lo.x, lo.y, hi.x, hi.y};
}
typedef f32x4 GAcc[4][4];
typedef v4u GFrag[4][2];
template <bool ZERO, int RK, int RP, int DK, int DP>
__device__ __forceinline__ void g_phase(const GCtx& c, const GStream& g, GAcc& acc, const GFrag& af, const GFrag& bf, GFrag& rf) {
#pragma unroll
  for (int n = 0; n < 32; ++n) {
    const int ks = n >> 4, nt = (n >> 2) & 3, mt = n & 3;
    if (ZERO && ks == 0) asm volatile("v_mfma_f32_16x16x32_bf16 %0, %1, %2, 0" : "=a"(acc[nt][mt]) : "v"(bf[nt][ks]), "v"(af[mt][ks]));
    else asm volatile("v_mfma_f32_16x16x32_bf16 %0, %1, %2, %0" : "+a"(acc[nt][mt]) : "v"(bf[nt][ks]), "v"(af[mt][ks]));
    if (n < 16 && (n & 1) == 0) {
      if (RK < 2) rf[(n >> 1) & 3][n >> 3] = g_rda<RK & 1, RP>(c, (n >> 1) & 3, n >> 3);
      else rf[(n >> 1) & 3][n >> 3] = g_rdb<RK & 1, RP>(c, (n >> 1) & 3, n >> 3);
    }
    if (n >= GDMA0 && n < GDMA0 + 4 * GDMAS && ((n - GDMA0) % GDMAS) == 0) g_dma<DK, DP>(c, g, (n - GDMA0) / GDMAS);
    if (n == GBAR) asm volatile("s_waitcnt vmcnt(24) lgkmcnt(0)\n\ts_barrier" ::: "memory");
    SB();
  }
}
template <bool FIRST>
__device__ __forceinline__ void g_pair(const GCtx& c, GStream& g, GAcc (&acc)[2][2], GFrag& a0, GFrag& a1, GFrag& bx, GFrag& by, bool sw, const GItem& nx,
                                       bool nvalid) {
  g_phase<FIRST, 3, 0, 2, 0>(c, g, acc[0][0], a0, bx, by);
  g_phase<FIRST, 1, 0, 3, 0>(c, g, acc[0][1], a0, by, a1);
  g.sw += 16 * g.rws;
  g_phase<FIRST, 0, 1, 1, 0>(c, g, acc[1][1], a1, by, a0);
  g.sa1 += 128;
  g_phase<FIRST, 2, 1, 0, 1>(c, g, acc[1][0], a1, bx, by);
  g.sa0 += g.ja0;
  if (sw) gs_set_a0(g, nx, nvalid, c.lane, c.w);
  g_phase<false, 3, 1, 2, 1>(c, g, acc[0][0], a0, by, bx);
  g_phase<false, 1, 1, 3, 1>(c, g, acc[0][1], a0, bx, a1);
  g.sw += 16 * g.rws;
  g_phase<false, 0, 0, 1, 1>(c, g, acc[1][1], a1, bx, a0);
  g.sa1 += g.ja1;
  g_phase<false, 2, 0, 0, 0>(c, g, acc[1][0], a1, by, bx);
  g.sa0 += 128;
  if (sw) gs_set_rest(g, nx, nvalid, c.lane, c.w);
}
template <int AUX, int NQ>
__device__ __forceinline__ void g_epilogue(const GCtx& c, const GItem& it, GAcc (&acc)[NQ][NQ]) {
  int lane = c.lane;
  NOPS(8);
  PIN1(lane);
  const int m = lane & 15, gq = lane >> 4, wr = c.w >> 1, wc = c.w & 1;
  LAS unsigned char* st = c.L + G_STAGE + c.w * 4096;
  __amdgpu_buffer_rsrc_t rc = mk_rsrc(it.C, ((NQ * 128 - 1) * it.ldc + (NQ - 1) * it.cjump + 128) * 2);
  const int vc = ((wr * 64 + (lane >> 3)) * it.ldc + wc * 64 + (lane & 7) * 8) * 2;
  const float sc = it.scale;
#pragma unroll
  for (int i = 0; i < NQ; ++i)
#pragma unroll
    for (int j = 0; j < NQ; ++j)
#pragma unroll
      for (int mh = 0; mh < 2; ++mh) {
#pragma unroll
        for (int mt = 2 * mh; mt < 2 * mh + 2; ++mt) {
          asm volatile("" : "+a"(acc[i][j][0][mt]), "+a"(acc[i][j][1][mt]), "+a"(acc[i][j][2][mt]), "+a"(acc[i][j][3][mt]));
#pragma unroll
          for (int nt = 0; nt < 4; ++nt) {
            const f32x4& v = acc[i][j][nt][mt];
            *(LAS v2u*)(st + ((mt & 1) * 16 + m) * 128 + (((nt * 2 + (gq >> 1)) ^ (m & 7)) * 16) + (gq & 1) * 8) =
                v2u{cvt_pk(v[0] * sc, v[1] * sc), cvt_pk(v[2] * sc, v[3] * sc)};
          }
        }
#pragma unroll
        for (int q = 0; q < 4; ++q) {
          const v4u o = *(LAS v4u*)(st + (8 * q + (lane >> 3)) * 128 + (((lane & 7) ^ (lane >> 3)) * 16));
          __builtin_amdgcn_raw_buffer_store_b128(o, rc, vc, ((i * 128 + mh * 32 + 8 * q) * it.ldc + j * it.cjump) * 2, AUX);
        }
        SB();
      }
}
__device__ __forceinline__ void g_publish(gu32* ctl, int flag, int two) {
  if (flag >= 0 && threadIdx.x == 0) {
    __hip_atomic_fetch_add(ctl + flag, two ? 2u : 1u, RLX_AGENT);
    if (two) __hip_atomic_fetch_add(ctl + flag + 64, 2u, RLX_AGENT);
  }
}
__device__ __forceinline__ void g_ctx(GCtx& c, LAS unsigned char* L) {
  c.L = L;
  c.lane = threadIdx.x & 63;
  PIN1(c.lane);
  c.w = __builtin_amdgcn_readfirstlane(threadIdx.x >> 6);
  c.ldsb = (unsigned)(size_t)L + c.w * 4096u;
  const int lane = c.lane, wr = c.w >> 1, wc = c.w & 1;
#pragma unroll
  for (int ks = 0; ks < 2; ++ks) c.aoff[ks] = (wr * 64 + (lane & 15)) * 128 + (((4 * ks + (lane >> 4)) ^ ((lane >> 1) & 7)) * 16);
#pragma unroll
  for (int nt = 0; nt < 4; ++nt)
    c.boff[nt] = 65536 + (8 * (lane >> 4) + ((lane >> 2) & 3)) * 256 +
                 (((8 * wc + 2 * nt + ((lane >> 1) & 1)) ^ (((lane >> 2) & 3) << 2) ^ (((lane >> 4) & 1) << 1)) * 16) + (lane & 1) * 8;
}
template <int AUX, class F>
__device__ __forceinline__ void gemm_tiles(LAS unsigned char* L, int n, F item, gu32* ctl) {
  if (n <= 0) return;
  GCtx c;
  g_ctx(c, L);
  const int lane = c.lane;
  GItem cur = item(0);
  GStream g;
  gs_set_a0(g, cur, true, lane, c.w);
  gs_set_rest(g, cur, true, lane, c.w);
  GFrag a0, a1, bx, by;
  GAcc acc[2][2];
  asm volatile("s_waitcnt vmcnt(0) lgkmcnt(0)\n\ts_barrier" ::: "memory");
#pragma unroll
  for (int i = 0; i < 4; ++i) g_dma<0, 0>(c, g, i);
#pragma unroll
  for (int i = 0; i < 4; ++i) g_dma<2, 0>(c, g, i);
#pragma unroll
  for (int i = 0; i < 4; ++i) g_dma<3, 0>(c, g, i);
#pragma unroll
  for (int i = 0; i < 4; ++i) g_dma<1, 0>(c, g, i);
  g.sa0 += 128; g.sa1 += 128; g.sw += 16 * g.rws;
#pragma unroll
  for (int i = 0; i < 4; ++i) g_dma<0, 1>(c, g, i);
#pragma unroll
  for (int i = 0; i < 4; ++i) g_dma<2, 1>(c, g, i);
#pragma unroll
  for (int i = 0; i < 4; ++i) g_dma<3, 1>(c, g, i);
#pragma unroll
  for (int i = 0; i < 4; ++i) g_dma<1, 1>(c, g, i);
  g.sa0 += g.ja0; g.sa1 += g.ja1; g.sw += 16 * g.rws;
  asm volatile("s_waitcnt vmcnt(24)\n\ts_barrier" ::: "memory");
#pragma unroll
  for (int k = 0; k < 8; ++k) { a0[k & 3][k >> 2] = g_rda<0, 0>(c, k & 3, k >> 2); bx[k & 3][k >> 2] = g_rdb<0, 0>(c, k & 3, k >> 2); }
  asm volatile("s_waitcnt lgkmcnt(0)\n\ts_barrier" ::: "memory");
#pragma unroll
  for (int i = 0; i < 4; ++i) g_dma<0, 0>(c, g, i);
  g.sa0 += 128;
  asm volatile("s_waitcnt vmcnt(24)\n\ts_barrier" ::: "memory");
  int pend = -1, pend2 = 0;
  for (int k = 0; k < n; ++k) {
    const bool nvalid = k + 1 < n;
    const GItem nx = item(nvalid ? k + 1 : k);
    g_pair<true>(c, g, acc, a0, a1, bx, by, cur.nk == 4, nx, nvalid);
    g_publish(ctl, pend, pend2);
    for (int t = 2; t < cur.nk; t += 2) g_pair<false>(c, g, acc, a0, a1, bx, by, t == cur.nk - 4, nx, nvalid);
    GSTAMP(0);
    g_epilogue<AUX, 2>(c, cur, acc);
    GSTAMP(1);
    pend = cur.flag;
    pend2 = cur.two;
    cur = nx;
  }
  asm volatile("s_waitcnt vmcnt(0) lgkmcnt(0)\n\ts_barrier" ::: "memory");
  g_publish(ctl, pend, pend2);
}

struct SStream { __amdgpu_buffer_rsrc_t ra, rw; int va[2], vw[2], sa, sw, ras, rws, ja; };
__device__ __forceinline__ void ss_set(SStream& g, const GItem& it, bool valid, int lane, int w) {
  g.ra = mk_rsrc(it.A, valid ? (127 * it.lda + (it.nk / 2 - 1) * (64 + it.ajump) + 128) * 2 : 0);
  g.rw = mk_rsrc(it.W, valid ? ((it.nk * 64 - 1) * it.ldw + 128) * 2 : 0);
#pragma unroll
  for (int e = 0; e < 2; ++e) {
    g.va[e] = (32 * w + 8 * e + (lane >> 3)) * it.lda * 2 + (((lane & 7) ^ (4 * e + (lane >> 4))) * 16);
    g.vw[e] = (16 * w + (lane >> 4)) * it.ldw * 2 + (((lane & 15) ^ ((lane >> 4) << 2) ^ (e << 1)) * 16);
  }
  g.sa = 0; g.sw = 0; g.ras = 32 * it.lda; g.rws = 8 * it.ldw; g.ja = it.ajump * 2;
}
template <int R>
__device__ __forceinline__ void s_dma(const GCtx& c, const SStream& g, int i) {
  if (i < 4) dma16(c.ldsb + R * 16384u + i * 1024u, g.va[i & 1], g.ra, g.sa + (i >> 1) * g.ras);
  else dma16(c.ldsb + 65536u + R * 16384u + (i - 4) * 1024u, g.vw[(i - 4) >> 1], g.rw, g.sw + (i - 4) * g.rws);
}
template <bool ZERO, int R>
__device__ __forceinline__ void s_step(const GCtx& c, SStream& g, GAcc& acc, const GFrag& af, const GFrag& bf, GFrag& an, GFrag& bn) {
  constexpr int N = (R + 1) & 3;
#pragma unroll
  for (int n = 0; n < 32; ++n) {
    const int ks = n >> 4, nt = (n >> 2) & 3, mt = n & 3;
    if (ZERO && ks == 0) asm volatile("v_mfma_f32_16x16x32_bf16 %0, %1, %2, 0" : "=a"(acc[nt][mt]) : "v"(bf[nt][ks]), "v"(af[mt][ks]));
    else asm volatile("v_mfma_f32_16x16x32_bf16 %0, %1, %2, %0" : "+a"(acc[nt][mt]) : "v"(bf[nt][ks]), "v"(af[mt][ks]));
    if (n < 24 && n % 3 == 0) an[(n / 3) & 3][(n / 3) >> 2] = g_rda<N & 1, (N >> 1)>(c, (n / 3) & 3, (n / 3) >> 2);
    if (n < 24 && n % 3 == 1) bn[(n / 3) & 3][(n / 3) >> 2] = g_rdb<N & 1, (N >> 1)>(c, (n / 3) & 3, (n / 3) >> 2);
    if (n < 24 && n % 3 == 2) s_dma<R>(c, g, n / 3);
    if (n == 29) asm volatile("s_waitcnt vmcnt(16) lgkmcnt(0)\n\ts_barrier" ::: "memory");
    SB();
  }
  g.sa += (R & 1) ? g.ja : 128;
  g.sw += 16 * g.rws;
}
template <int AUX, class F>
__device__ __forceinline__ void gemm_small(LAS unsigned char* L, int n, F item, gu32* ctl) {
  if (n <= 0) return;
  GCtx c;
  g_ctx(c, L);
  GItem cur = item(0);
  SStream g;
  ss_set(g, cur, true, c.lane, c.w);
  GFrag a0, a1, b0, b1;
  GAcc acc[1][1];
  asm volatile("s_waitcnt vmcnt(0) lgkmcnt(0)\n\ts_barrier" ::: "memory");
#pragma unroll
  for (int i = 0; i < 8; ++i) s_dma<0>(c, g, i);
  g.sa += 128; g.sw += 16 * g.rws;
#pragma unroll
  for (int i = 0; i < 8; ++i) s_dma<1>(c, g, i);
  g.sa += g.ja; g.sw += 16 * g.rws;
#pragma unroll
  for (int i = 0; i < 8; ++i) s_dma<2>(c, g, i);
  g.sa += 128; g.sw += 16 * g.rws;
#pragma unroll
  for (int i = 0; i < 8; ++i) s_dma<3>(c, g, i);
  g.sa += g.ja; g.sw += 16 * g.rws;
  asm volatile("s_waitcnt vmcnt(24)\n\ts_barrier" ::: "memory");
#pragma unroll
  for (int k = 0; k < 8; ++k) { a0[k & 3][k >> 2] = g_rda<0, 0>(c, k & 3, k >> 2); b0[k & 3][k >> 2] = g_rdb<0, 0>(c, k & 3, k >> 2); }
  asm volatile("s_waitcnt vmcnt(16) lgkmcnt(0)\n\ts_barrier" ::: "memory");
  int pend = -1;
  for (int k = 0; k < n; ++k) {
    const bool nvalid = k + 1 < n;
    const GItem nx = item(nvalid ? k + 1 : k);
    s_step<true, 0>(c, g, acc[0][0], a0, b0, a1, b1);
    s_step<false, 1>(c, g, acc[0][0], a1, b1, a0, b0);
    s_step<false, 2>(c, g, acc[0][0], a0, b0, a1, b1);
    g_publish(ctl, pend, 0);
    s_step<false, 3>(c, g, acc[0][0], a1, b1, a0, b0);
    for (int t = 4; t < cur.nk; t += 4) {
      if (t == cur.nk - 4) ss_set(g, nx, nvalid, c.lane, c.w);
      s_step<false, 0>(c, g, acc[0][0], a0, b0, a1, b1);
      s_step<false, 1>(c, g, acc[0][0], a1, b1, a0, b0);
      s_step<false, 2>(c, g, acc[0][0], a0, b0, a1, b1);
      s_step<false, 3>(c, g, acc[0][0], a1, b1, a0, b0);
    }
    g_epilogue<AUX, 1>(c, cur, acc);
    SSTAMP();
    pend = cur.flag;
    if (k == 0 && nvalid) {
      asm volatile("s_waitcnt vmcnt(0)\n\ts_barrier" ::: "memory");
      g_publish(ctl, pend, 0);
      pend = -1;
    }
    cur = nx;
  }
  asm volatile("s_waitcnt vmcnt(0) lgkmcnt(0)\n\ts_barrier" ::: "memory");
  g_publish(ctl, pend, 0);
}

#define ASTAMP(k)
#define MF32_C(d, a, b, c) asm volatile("v_mfma_f32_32x32x16_bf16 %0, %1, %2, %3" : "=&v"(d) : "v"(a), "v"(b), "v"(c))
#define MF32_V(d, a, b) asm volatile("v_mfma_f32_32x32x16_bf16 %0, %1, %2, %0" : "+v"(d) : "v"(a), "v"(b))
#define MF32_A(d, a, b) asm volatile("v_mfma_f32_32x32x16_bf16 %0, %1, %2, %0" : "+a"(d) : "v"(a), "v"(b))
struct ACtx {
  LAS unsigned char* L;
  unsigned ldsb;
  __amdgpu_buffer_rsrc_t rkv;
  int vk[2], vv;
  int koff[2], voff;
  v4u q[2][2], ones;
  v4u kf[2][2], vf[2][2], pb[2][2];
  f32x16 s[2], negr[2], o[2], l[2];
  float t[4];
};
__device__ __forceinline__ void a_dma(ACtx& c, int stage, int i) {
  const unsigned m0v = c.ldsb + (stage & 3) * 32768 + (i >> 2) * 16384 + (i & 3) * 1024;
  dma16(m0v, i < 4 ? c.vk[i & 1] : c.vv, c.rkv, stage * (128 * 6144) + (i < 4 ? (i >> 1) * 16 * 6144 : 2048 + (i & 3) * 8 * 6144));
}
__device__ __forceinline__ v4u a_rdk(ACtx& c, int ko, int blk) { return *(LAS v4u*)(c.L + ko + blk * 4096); }
__device__ __forceinline__ v4u a_rdv(ACtx& c, int vo_, int blk, int s) {
  LAS unsigned char* p = c.L + vo_ + blk * 4096 + s * 2048;
  const v2u lo = trread(p), hi = trread(p + 1024);
  return v4u{lo.x, lo.y, hi.x, hi.y};
}
template <int QT>
__device__ __forceinline__ void a_ref(ACtx& c) {
  float mx = c.s[QT][0];
#pragma unroll
  for (int r = 1; r < 16; ++r) mx = fmx(mx, c.s[QT][r]);
  auto sw = __builtin_amdgcn_permlane32_swap(ubits(mx), ubits(mx), false, false);
  const float d = fmx(fbits(sw[0]), fbits(sw[1]));
  c.negr[QT] -= d;
  c.s[QT] -= d;
  asm volatile("s_nop 1" : "+v"(c.negr[QT]), "+v"(c.s[QT]));
}
template <int QT, int BLK, int MODE>
__device__ __forceinline__ void a_tile(ACtx& c, int k0, int k1, int v0, int dstage) {
  constexpr int P = QT ^ 1, KN = (BLK + QT) & 1, V1 = (BLK + QT + 1) & 1, V2 = (BLK + 1) & 1;
  constexpr bool H1 = MODE & 1, H2 = MODE & 2, H0 = MODE & 4;
  float e[16];
  PIN1(c.s[QT]);
#pragma unroll
  for (int i = 0; i < 8; ++i) {
    if (i == 0 && H2) MF32_A(c.o[QT], c.vf[V2][1], c.pb[QT][1]);
    if (i == AP1 && H2 && !(ABL & 2)) MF32_A(c.l[QT], c.ones, c.pb[QT][1]);
    if (i == AP2 && H0) MF32_C(c.s[P], c.kf[KN][0], c.q[P][0], c.negr[P]);
    if (i == AP3 && H0) MF32_V(c.s[P], c.kf[KN][1], c.q[P][1]);
    if (i == AP4 && H1) MF32_A(c.o[P], c.vf[V1][0], c.pb[P][0]);
    if (i == AP5 && H1 && !(ABL & 2)) MF32_A(c.l[P], c.ones, c.pb[P][0]);
    SB();
    if (H0) { e[2 * i] = __builtin_amdgcn_exp2f(c.s[QT][2 * i]); if (i >= 6) PIN1(e[2 * i]); SB(); }
    if (i >= 2) { if (H0) c.pb[QT][(i - 2) >> 2][(i - 2) & 3] = cvt_pk(e[2 * i - 4], e[2 * i - 3]); }
    else if (H1) c.pb[P][1][2 + i] = cvt_pk(c.t[2 * i], c.t[2 * i + 1]);
    SB();
    if (H0) { e[2 * i + 1] = __builtin_amdgcn_exp2f(c.s[QT][2 * i + 1]); if (i >= 6) PIN1(e[2 * i + 1]); SB(); }
    if (QT == 0 && H0) {
      if (i == 2) c.kf[(BLK + 1) & 1][0] = a_rdk(c, k0, (BLK + 1) & 3);
      if (i == 4) c.kf[(BLK + 1) & 1][1] = a_rdk(c, k1, (BLK + 1) & 3);
      if (i == 6) c.vf[BLK & 1][0] = a_rdv(c, v0, BLK, 0);
    }
    if (QT == 1 && H0) {
      if (i == 2) c.vf[BLK & 1][1] = a_rdv(c, v0, BLK, 1);
    }
    if (H0 && i == (QT ? 6 : 1) && !(ABL & 16)) a_dma(c, dstage, 2 * BLK + QT);
    SB();
  }
  if (H0) {
#pragma unroll
    for (int j = 0; j < 4; ++j) c.t[j] = e[12 + j];
  }
}
template <int AUX>
__device__ __forceinline__ void a_store(u16* O, const f32x16 (&o)[2], const f32x16 (&l)[2], int el) {
  __amdgpu_buffer_rsrc_t ro = mk_rsrc(O, 2048 * 1024 * 2);
#pragma unroll
  for (int qt = 0; qt < 2; ++qt) {
    const float ri = __builtin_amdgcn_rcpf(l[qt][0]);
    unsigned d[8];
#pragma unroll
    for (int i = 0; i < 8; ++i) d[i] = cvt_pk(o[qt][2 * i] * ri, o[qt][2 * i + 1] * ri);
#pragma unroll
    for (int k = 0; k < 2; ++k) {
      auto x = __builtin_amdgcn_permlane32_swap(d[4 * k], d[4 * k + 2], false, false);
      auto y = __builtin_amdgcn_permlane32_swap(d[4 * k + 1], d[4 * k + 3], false, false);
      __builtin_amdgcn_raw_buffer_store_b128(v4u{x[0], y[0], x[1], y[1]}, ro, (el + qt * 32 * 1024 + k * 16) * 2, 0, AUX);
    }
  }
}
__device__ __forceinline__ void attn_safe(ACtx& c, f32x16 (&o)[2], f32x16 (&l)[2]) {
  float m[2] = {-3.0e38f, -3.0e38f};
#pragma unroll
  for (int qt = 0; qt < 2; ++qt)
#pragma unroll
    for (int r = 0; r < 16; ++r) { o[qt][r] = 0.f; l[qt][r] = 0.f; }
  for (int b = 0; b < 64; ++b) {
    if ((b & 3) == 0) {
      asm volatile("s_waitcnt lgkmcnt(0)\n\ts_barrier" ::: "memory");
      for (int i = 0; i < 8; ++i) {
        const unsigned m0v = c.ldsb + (i >> 2) * 16384 + (i & 3) * 1024;
        dma16(m0v, i < 4 ? ((i & 1) ? c.vk[1] : c.vk[0]) : c.vv, c.rkv, (b >> 2) * (128 * 6144) + (i < 4 ? (i >> 1) * 16 * 6144 : 2048 + (i & 3) * 8 * 6144));
      }
      asm volatile("s_waitcnt vmcnt(0)\n\ts_barrier" ::: "memory");
    }
    const v4u ka = a_rdk(c, c.koff[0], b & 3), kb = a_rdk(c, c.koff[1], b & 3), va = a_rdv(c, c.voff, b & 3, 0), vb = a_rdv(c, c.voff, b & 3, 1);
#pragma unroll
    for (int qt = 0; qt < 2; ++qt) {
      f32x16 s;
#pragma unroll
      for (int r = 0; r < 16; ++r) s[r] = 0.f;
      s = mfma32(kb, c.q[qt][1], mfma32(ka, c.q[qt][0], s));
      float mx = m[qt];
#pragma unroll
      for (int r = 0; r < 16; ++r) mx = fmx(mx, s[r]);
      auto sw = __builtin_amdgcn_permlane32_swap(ubits(mx), ubits(mx), false, false);
      mx = fmx(fbits(sw[0]), fbits(sw[1]));
      const float f = __builtin_amdgcn_exp2f(m[qt] - mx);
      m[qt] = mx;
      v4u p[2];
#pragma unroll
      for (int i = 0; i < 8; ++i) p[i >> 2][i & 3] = cvt_pk(__builtin_amdgcn_exp2f(s[2 * i] - mx), __builtin_amdgcn_exp2f(s[2 * i + 1] - mx));
      o[qt] = mfma32(vb, p[1], mfma32(va, p[0], o[qt] * f));
      l[qt] = mfma32(c.ones, p[1], mfma32(c.ones, p[0], l[qt] * f));
    }
  }
}
template <int AUX>
__device__ __forceinline__ void attn_item(LAS unsigned char* L, const u16* QKV, u16* O, int hp, int qb) {
  ACtx c;
  ASTAMP(0);
  int lane = threadIdx.x & 63;
  PIN1(lane);
  const int w = __builtin_amdgcn_readfirstlane(threadIdx.x >> 6), hs = w >> 1, hh = lane >> 5;
  c.L = L;
  c.ldsb = (unsigned)(size_t)L + w * 4096u;
  c.rkv = mk_rsrc(QKV + 1024 + hp * 64, (2047 * 3072 + 1024 + 64) * 2);
#pragma unroll
  for (int e = 0; e < 2; ++e) c.vk[e] = (32 * w + 8 * e + (lane >> 3)) * 6144 + (((lane & 7) ^ (4 * e + (lane >> 4))) * 16);
  c.vv = (32 * w + (lane >> 3)) * 6144 + (((lane & 7) ^ (((lane >> 4) & 1) << 2)) * 16);
#pragma unroll
  for (int ks = 0; ks < 2; ++ks) c.koff[ks] = (lane & 31) * 128 + (((hs * 4 + 2 * ks + hh) ^ ((lane >> 1) & 7)) * 16);
  c.voff = 16384 + (4 * hh + ((lane >> 2) & 3)) * 128 + (((hs * 4 + ((lane >> 4) & 1) * 2 + ((lane >> 1) & 1)) ^ (((lane >> 3) & 1) << 2)) * 16) + (lane & 1) * 8;
  asm volatile("s_waitcnt vmcnt(0) lgkmcnt(0)\n\ts_barrier" ::: "memory");
  const int q0 = qb * 128 + (w & 1) * 64;
#pragma unroll
  for (int qt = 0; qt < 2; ++qt)
#pragma unroll
    for (int ks = 0; ks < 2; ++ks) c.q[qt][ks] = *(const GAS v4u*)(QKV + (size_t)(q0 + qt * 32 + (lane & 31)) * 3072 + hp * 64 + hs * 32 + ks * 16 + hh * 8);
  SB();
#pragma unroll
  for (int st = 0; st < 2; ++st)
#pragma unroll
    for (int i = 0; i < 8; ++i) a_dma(c, st, i);
  c.ones = v4u{0x3F803F80u, 0x3F803F80u, 0x3F803F80u, 0x3F803F80u};
#pragma unroll
  for (int qt = 0; qt < 2; ++qt) {
#pragma unroll
    for (int r = 0; r < 16; ++r) { c.negr[qt][r] = 0.f; c.o[qt][r] = 0.f; c.l[qt][r] = 0.f; }
    asm volatile("" : "+v"(c.negr[qt]), "+a"(c.o[qt]), "+a"(c.l[qt]));
  }
  PIN1(c.ones);
  asm volatile("s_waitcnt vmcnt(8)\n\ts_barrier" : "+v"(c.q[0][0]), "+v"(c.q[0][1]), "+v"(c.q[1][0]), "+v"(c.q[1][1])::"memory");
  c.kf[0][0] = a_rdk(c, c.koff[0], 0);
  c.kf[0][1] = a_rdk(c, c.koff[1], 0);
  MF32_C(c.s[1], c.kf[0][0], c.q[1][0], c.negr[1]);
  MF32_V(c.s[1], c.kf[0][1], c.q[1][1]);
  MF32_C(c.s[0], c.kf[0][0], c.q[0][0], c.negr[0]);
  MF32_V(c.s[0], c.kf[0][1], c.q[0][1]);
  NOPS(12);
  PIN2(c.s[0], c.s[1]);
  a_ref<1>(c);
  a_ref<0>(c);
  ASTAMP(1);
  a_tile<0, 0, 4>(c, c.koff[0], c.koff[1], c.voff, 2);
  a_tile<1, 0, 5>(c, c.koff[0], c.koff[1], c.voff, 2);
  auto six = [&](int j) {
    const int so = (j & 3) * 32768, sn = ((j + 1) & 3) * 32768, ds = min(j + 2, 15);
    const int k0 = c.koff[0] + so, k1 = c.koff[1] + so, v0 = c.voff + so;
    a_tile<0, 1, 7>(c, k0, k1, v0, ds);
    a_tile<1, 1, 7>(c, k0, k1, v0, ds);
    a_tile<0, 2, 7>(c, k0, k1, v0, ds);
    if (!(ABL & 8)) asm volatile("s_waitcnt vmcnt(5)\n\ts_barrier" ::: "memory");
    a_tile<1, 2, 7>(c, k0, k1, v0, ds);
    a_tile<0, 3, 7>(c, c.koff[0] + sn, c.koff[1] + sn, v0, ds);
    a_tile<1, 3, 7>(c, k0, k1, v0, ds);
  };
  for (int j = 0; j < 15; ++j) {
    six(j);
    const int sn = ((j + 1) & 3) * 32768;
    a_tile<0, 0, 7>(c, c.koff[0] + sn, c.koff[1] + sn, c.voff + sn, min(j + 3, 15));
    a_tile<1, 0, 7>(c, c.koff[0] + sn, c.koff[1] + sn, c.voff + sn, min(j + 3, 15));
  }
  six(15);
  a_tile<0, 0, 3>(c, 0, 0, 0, 0);
  NOPS(2);
  MF32_A(c.o[1], c.vf[1][1], c.pb[1][1]);
  MF32_A(c.l[1], c.ones, c.pb[1][1]);
  NOPS(12);
  asm volatile("" : "+a"(c.o[0]), "+a"(c.l[0]), "+a"(c.o[1]), "+a"(c.l[1]), "+v"(c.s[0]), "+v"(c.negr[0]), "+v"(c.negr[1]));
  ASTAMP(2);
  bool bad = false;
#pragma unroll
  for (int qt = 0; qt < 2; ++qt) {
    float m = c.l[qt][0];
#pragma unroll
    for (int r = 0; r < 16; ++r) m = fmx(m, __builtin_fabsf(c.o[qt][r]));
    bad |= !(m < 1e30f);
  }
  LAS unsigned* vote = (LAS unsigned*)(L + G_STAGE);
  if (lane == 0) vote[w] = 0u;
  if (bad) vote[w] = 1u;
  asm volatile("s_waitcnt vmcnt(0) lgkmcnt(0)\n\ts_barrier" ::: "memory");
  const v4u vt = *(LAS v4u*)vote;
  if (FORCE_SAFE || __builtin_amdgcn_readfirstlane(vt.x | vt.y | vt.z | vt.w)) attn_safe(c, c.o, c.l);
  a_store<AUX>(O, c.o, c.l, (q0 + (lane & 31)) * 1024 + hp * 64 + hs * 32 + hh * 8);
  ASTAMP(3);
}

struct Args {
  const u16* Ax; const u16* Wq; const u16* Wk; const u16* Wv; const u16* Wo; const u16* Bx; const u16* Wb; const u16* Wob;
  u16* out; unsigned char* base;
};
constexpr float CB = 0.17677669529663687f * 1.4426950408889634f;
__device__ __forceinline__ GItem proj_item(const Args& a, int x, int id) {
  GItem it;
  it.lda = 1024; it.nk = 16; it.scale = 1.f; it.ajump = 64; it.cjump = 128;
  if (id < 96) {
    const int rb = id / 12, ct = (id % 12) * 4 + x, m = ct >> 4;
    it.A = a.Ax + (size_t)rb * 256 * 1024;
    it.W = (m == 0 ? a.Wq : m == 1 ? a.Wk : a.Wv) + (ct & 15) * 256;
    it.ldw = 4096;
    it.C = (u16*)(a.base + XQKV_OFF) + ((size_t)2 * ct * SEQ + rb * 256) * 128;
    it.ldc = 128;
    it.cjump = SEQ * 128;
    if (m == 0) it.scale = CEXP;
    it.flag = CTL_RB + 128 * rb;
    it.two = 1;
  } else {
    const int rb = (id - 96) / 3, ct = ((id - 96) % 3) * 4 + x;
    it.A = a.Bx + (size_t)rb * 256 * 1024;
    it.W = a.Wb + ct * 256;
    it.ldw = 3072;
    it.C = (u16*)(a.base + BQKV_OFF) + (size_t)rb * 256 * 3072 + ct * 256;
    it.ldc = 3072;
    if (ct < 4) it.scale = CB;
    it.flag = CTL_B;
    it.two = 0;
  }
  return it;
}
__device__ __forceinline__ GItem small_item(const Args& a, int x, int i, int k) {
  const int id = k < 2 ? i >> 1 : (k - 1) * 12 + 8 + (i >> 2), rh = k < 2 ? k : (i >> 1) & 1, ch = i & 1;
  GItem it = proj_item(a, x, id);
  it.A += rh * 128 * 1024;
  it.W += ch * 128;
  it.C += (size_t)(rh * 128 + ch * SEQ) * 128;
  it.flag += 64 * rh;
  it.two = 0;
  return it;
}
__device__ __forceinline__ int big_id(int i, int k) {
  if (i < 16) return k == 0 ? 44 + i : k == 1 ? (i < 8 ? 112 + i : 52 + i) : 80 + i;
  if (i < 24) return k == 0 ? 8 + i : k == 1 ? 80 + i : 52 + i;
  return k == 0 ? i - 12 : k == 1 ? 12 + i : k == 2 ? 80 + i : 52 + i;
}
__device__ __forceinline__ void wg_wait(gu32* ctl, int word, unsigned want) {
  if (threadIdx.x < 64) {
    bool dead = false;
    for (unsigned spins = 0; __builtin_amdgcn_readfirstlane(__hip_atomic_load(ctl + word, RLX_AGENT)) < want;) {
      __builtin_amdgcn_s_sleep(16);
      if (spin_fail(spins, ctl, dead)) break;
    }
    __builtin_amdgcn_fence(__ATOMIC_ACQUIRE, "agent");
    asm volatile("s_waitcnt vmcnt(0)" ::: "memory");
  }
  asm volatile("s_barrier" ::: "memory");
}
__device__ __forceinline__ int wg_next(LAS unsigned char* L, gu32* ctl, int done, int queue) {
  LAS int* slot = (LAS int*)(L + G_STAGE + 64);
  asm volatile("s_waitcnt vmcnt(0) lgkmcnt(0)\n\ts_barrier" ::: "memory");
  if (threadIdx.x == 0) {
    if (done >= 0) __hip_atomic_fetch_add(ctl + done, 1u, RLX_AGENT);
    *slot = (int)__hip_atomic_fetch_add(ctl + queue, 1u, RLX_AGENT);
  }
  asm volatile("s_waitcnt vmcnt(0) lgkmcnt(0)\n\ts_barrier" ::: "memory");
  return __builtin_amdgcn_readfirstlane(*slot);
}
template <int N>
__device__ __forceinline__ v4u sum_bf16(const v4u (&v)[N]) {
  v4u o;
#pragma unroll
  for (int j = 0; j < 4; ++j) {
    float lo = 0.f, hi = 0.f;
#pragma unroll
    for (int k = 0; k < N; ++k) { lo += fbits(v[k][j] << 16); hi += fbits(v[k][j] & 0xFFFF0000u); }
    o[j] = cvt_pk(lo, hi);
  }
  return o;
}
#define OSTAMP(k)
__device__ __forceinline__ void others_role(const Args& a, LAS unsigned char* L, int x, int i) {
  gu32* ctl = (gu32*)(a.base + CTL_OFF);
  gemm_small<AUX_SC1>(L, i < 16 ? 4 : i < 24 ? 2 : 0, [&](int k) { return small_item(a, x, i, k); }, ctl);
  gemm_tiles<AUX_SC1>(L, (i >= 24 && i < 28) ? 4 : 3, [&](int k) { return proj_item(a, x, big_id(i, k)); }, ctl);
  OSTAMP(1);
  wg_wait(ctl, CTL_B, 96u);
  int done = -1;
  for (int q = 0; q < 4; ++q) {
    const int xq = (x + q) & 3;
    for (;;) {
      const int k = wg_next(L, ctl, done, CTL_AQ + 64 * xq);
      done = -1;
      if (k >= 64) break;
      attn_item<AUX_SC1>(L, (const u16*)(a.base + BQKV_OFF), (u16*)(a.base + BO_OFF), xq * 4 + (k & 3), k >> 2);
      done = CTL_ATT + 64 * (k >> 3);
    }
  }
  OSTAMP(2);
  const int k = wg_next(L, ctl, -1, CTL_GQ);
  if (k < 128) {
    wg_wait(ctl, CTL_ATT + 64 * (k >> 4), 32u);
    gemm_small<0>(L, 1, [&](int) {
      GItem it;
      it.A = (const u16*)(a.base + BO_OFF) + (size_t)(k >> 3) * 128 * 1024; it.lda = 1024;
      it.W = a.Wob + (k & 7) * 128; it.ldw = 1024;
      it.C = a.out + (size_t)(k >> 3) * 128 * 2048 + 1024 + (k & 7) * 128; it.ldc = 2048;
      it.nk = 16; it.scale = 1.f; it.flag = -1; it.two = 0; it.ajump = 64; it.cjump = 128;
      return it; }, ctl);
  }
  const int u = wg_next(L, ctl, -1, CTL_OQ);
  if (u < NRB1 * 16) {
    const int t = u >> 2, kq = u & 3;
    u16* slab = (u16*)(a.base + SLAB_OFF) + (size_t)t * 4 * 65536;
    wg_wait(ctl, CTL_OX + 64 * ((t >> 2) * 4 + kq), 32u);
    gemm_tiles<AUX_SC1>(L, 1, [&](int) {
      GItem it;
      it.A = (const u16*)(a.base + OX_OFF) + ((size_t)kq * 8 * SEQ + (t >> 2) * 256) * 128; it.lda = 128;
      it.W = a.Wo + (size_t)kq * 1024 * 1024 + (t & 3) * 256; it.ldw = 1024;
      it.C = slab + kq * 65536; it.ldc = 256;
      it.nk = 16; it.scale = 1.f; it.flag = -1; it.two = 0; it.ajump = SEQ * 128 - 64; it.cjump = 128;
      return it; }, ctl);
    if (wg_next(L, ctl, -1, CTL_O4 + 64 * t) == 3) {
      wg_wait(ctl, CTL_O4 + 64 * t, 4u);
      for (int r = 0; r < 8; ++r) {
        v4u v[4][4];
#pragma unroll
        for (int q = 0; q < 4; ++q)
#pragma unroll
          for (int k = 0; k < 4; ++k) v[q][k] = ((const GAS v4u*)slab)[k * 8192 + (r * 4 + q) * 256 + threadIdx.x];
#pragma unroll
        for (int q = 0; q < 4; ++q)
          *(GAS v4u*)(a.out + (size_t)((t >> 2) * 256 + (r * 4 + q) * 8 + (threadIdx.x >> 5)) * 2048 + (t & 3) * 256 + (threadIdx.x & 31) * 8) = sum_bf16<4>(v[q]);
      }
    }
  }
  OSTAMP(3);
}
__global__ void __launch_bounds__(256, 1) k_main(Args a) {
  extern __shared__ __attribute__((aligned(16))) unsigned char lds[];
  const int b = blockIdx.x;
  OSTAMP(0);
  if ((b & 7) < 4) {
    ChainArgs ca{a.Wq, a.Wk, a.Wv, a.Wo, a.base};
    chain_role(ca, (LAS unsigned char*)lds, (b >> 3) * 4 + (b & 3));
    OSTAMP(3);
  } else {
    others_role(a, (LAS unsigned char*)lds, b & 3, b >> 3);
  }
}
__global__ void __launch_bounds__(256, 1) k_outA(Args a) {
  extern __shared__ __attribute__((aligned(16))) unsigned char lds[];
  const int b = blockIdx.x, kq = b & 7, t = b >> 3, row0 = NRB1 * 256 + (t >> 3) * 128;
  gu32* ctl = (gu32*)(a.base + CTL_OFF);
  u16* slab = (u16*)(a.base + SLAB_OFF) + (size_t)NRB1 * 16 * 65536 + (size_t)t * 8 * 16384;
  OSTAMP(4);
  gemm_small<AUX_SC1>((LAS unsigned char*)lds, 1, [&](int) {
    GItem it;
    it.A = (const u16*)(a.base + OX_OFF) + ((size_t)kq * 4 * SEQ + row0) * 128; it.lda = 128;
    it.W = a.Wo + (size_t)kq * 512 * 1024 + (t & 7) * 128; it.ldw = 1024;
    it.C = slab + kq * 16384; it.ldc = 128;
    it.nk = 8; it.scale = 1.f; it.flag = CTL_OT + 64 * t; it.two = 0; it.ajump = SEQ * 128 - 64; it.cjump = 128;
    return it; }, ctl);
  OSTAMP(5);
  wg_wait(ctl, CTL_OT + 64 * t, 8u);
  OSTAMP(6);
  v4u v[8];
#pragma unroll
  for (int k = 0; k < 8; ++k) v[k] = ((const GAS v4u*)slab)[k * 2048 + kq * 256 + threadIdx.x];
  *(GAS v4u*)(a.out + (size_t)(row0 + kq * 16 + (threadIdx.x >> 4)) * 2048 + (t & 7) * 128 + (threadIdx.x & 15) * 8) = sum_bf16<8>(v);
  OSTAMP(7);
}

}

extern "C" void kernel_launch(void* const* d_in, const int* in_sizes, int n_in, void* d_out, int out_size, void* d_ws, size_t ws_size,
                              hipStream_t stream) {
  unsigned char* base = (unsigned char*)(((uintptr_t)d_ws + (2u << 20) - 1) & ~(uintptr_t)((2u << 20) - 1));
  Args a{(const u16*)d_in[0], (const u16*)d_in[2], (const u16*)d_in[3], (const u16*)d_in[4], (const u16*)d_in[5],
         (const u16*)d_in[7], (const u16*)d_in[8], (const u16*)d_in[9], (u16*)d_out, base};
  (void)hipFuncSetAttribute((const void*)k_main, hipFuncAttributeMaxDynamicSharedMemorySize, GEMM_LDS);
  (void)hipFuncSetAttribute((const void*)k_outA, hipFuncAttributeMaxDynamicSharedMemorySize, GEMM_LDS);
  k_init<<<256, 256, 0, stream>>>(base, (const u16*)d_in[1]);
  k_main<<<256, 256, GEMM_LDS, stream>>>(a);
  k_outA<<<256, 256, GEMM_LDS, stream>>>(a);
}
```

```cpp
#include <hip/hip_runtime.h>
#include <hip/hip_bf16.h>
#include <stdint.h>
#include <stdio.h>
#include <type_traits>

#define LAS __attribute__((address_space(3)))
#define GAS __attribute__((address_space(1)))
typedef unsigned v4u __attribute__((ext_vector_type(4)));
typedef unsigned v2u __attribute__((ext_vector_type(2)));
typedef unsigned v8u __attribute__((ext_vector_type(8)));
typedef float f32x4 __attribute__((ext_vector_type(4)));
typedef float f32x2 __attribute__((ext_vector_type(2)));
typedef float f32x16 __attribute__((ext_vector_type(16)));
typedef __bf16 bf16x8 __attribute__((ext_vector_type(8)));
typedef __bf16 bf16x16 __attribute__((ext_vector_type(16)));
typedef __bf16 bf16x2 __attribute__((ext_vector_type(2)));
typedef short v4i16 __attribute__((ext_vector_type(4)));
typedef unsigned short u16;
typedef GAS unsigned gu32;
#define RLX_AGENT __ATOMIC_RELAXED, __HIP_MEMORY_SCOPE_AGENT

#define DBG_LITE 0
#define DBG_STAMP 0

namespace {

constexpr int SEQ = 2048, DM = 1024, NH = 32, DH = 128, NSEG = 64;
constexpr int LDX = 3 * NH * DH;
constexpr size_t COMM_BYTES = 6u << 20;
constexpr size_t CTL_OFF = COMM_BYTES;
constexpr size_t XQKV_OFF = CTL_OFF + (2u << 20);
constexpr size_t BQKV_OFF = XQKV_OFF + (size_t)SEQ * LDX * 4;
constexpr size_t SLAB_OFF = BQKV_OFF + (size_t)SEQ * 3072 * 4;
constexpr size_t SLAB2_OFF = SLAB_OFF + (size_t)96 * 65536 * 4;
constexpr size_t BO_OFF = SLAB2_OFF + (size_t)448 * 16384 * 4;
constexpr size_t WS_END = BO_OFF + (size_t)SEQ * 1024 * 2 + 4096;
static_assert(XQKV_OFF % (2u << 20) == 0 && BQKV_OFF % (2u << 20) == 0 && SLAB_OFF % (2u << 20) == 0 && SLAB2_OFF % (2u << 20) == 0 && WS_END + (2u << 20) <= (256u << 20), "workspace map");
constexpr int CTL_POL = 0, CTL_ABORT = 64, CTL_RB = 128  , CTL_B = 1152, CTL_ATT = 1216  , CTL_AQ = 1728  , CTL_GQ = 1984, CTL_OT = 2048  , CTL_OX = 4096  ,
              CTL_O4 = 6656  , CTL_OQ = 8192, CTL_LQ = 8256, CTL_WORDS = 8320,
              CTL_CLAIM = 8320  ;
constexpr int NRB1 = 6;
constexpr int NLAST = 3;
constexpr int CTL_STAMP = 16384;
constexpr float CEXP = 0.08838834764831845f * 1.4426950408889634f;
#define FORCE_SAFE 0
constexpr unsigned SENT = 0xFFFFFFFFu;
constexpr int AUX_SC1 = 16;
constexpr unsigned SPIN_CAP = 1u << 16;
constexpr int MEM_SLOT = 2 * 65536, QKV_BASE = 2 * 262144, QKV_SLOT = 2 * 196608;

__device__ __forceinline__ int far_off(unsigned pol, int window) { return (int)(((pol ^ 1u ^ (unsigned)window) & 1u) << 13); }
__device__ __forceinline__ int vo(int o) { return ((o >> 13) << 14) | (o & 8191); }
__device__ __forceinline__ unsigned cvt_pk(float lo, float hi) {
  f32x2 v = {lo, hi};
  return __builtin_bit_cast(unsigned, __builtin_convertvector(v, bf16x2));
}
__device__ __forceinline__ f32x4 mfma16(v4u a, v4u b, f32x4 c) {
  return __builtin_amdgcn_mfma_f32_16x16x32_bf16(__builtin_bit_cast(bf16x8, a), __builtin_bit_cast(bf16x8, b), c, 0, 0, 0);
}
__device__ __forceinline__ f32x4 smfmac(v4u a, v8u b, f32x4 c, int idx) {
  return __builtin_amdgcn_smfmac_f32_16x16x64_bf16(__builtin_bit_cast(bf16x8, a), __builtin_bit_cast(bf16x16, b), c, idx, 0, 0);
}
#define MF_Z(d, a, b, BC) asm volatile("v_mfma_f32_16x16x32_bf16 %0, %1, %2, 0" : "=&v"(d) : "v"(a), BC(b))
#define MF_C(d, a, b, c) asm volatile("v_mfma_f32_16x16x32_bf16 %0, %1, %2, %3" : "=&v"(d) : "v"(a), "v"(b), "v"(c))
#define MF_A(d, a, b, BC) asm volatile("v_mfma_f32_16x16x32_bf16 %0, %1, %2, %0" : "+v"(d) : "v"(a), BC(b))
#define SMF_A(d, a, b, i) asm volatile("v_smfmac_f32_16x16x64_bf16 %0, %1, %2, %3" : "+v"(d) : "v"(a), "a"(b), "v"(i))
#define NOPS(n) asm volatile("s_nop %0" ::"i"((n)-1))
#define PIN1(a) asm volatile("" : "+v"(a))
#define PIN2(a, b) asm volatile("" : "+v"(a), "+v"(b))
__device__ __forceinline__ v2u trread(LAS unsigned char* p) {
  return __builtin_bit_cast(v2u, __builtin_amdgcn_ds_read_tr16_b64_v4i16((LAS v4i16*)p));
}
__device__ __forceinline__ unsigned umax3(unsigned a, unsigned b, unsigned c) { return max(max(a, b), c); }
__device__ __forceinline__ unsigned umax4(unsigned m, v4u v) { return umax3(umax3(m, v.x, v.y), v.z, v.w); }
__device__ __forceinline__ float fbits(unsigned u) { return __builtin_bit_cast(float, u); }
__device__ __forceinline__ unsigned ubits(float f) { return __builtin_bit_cast(unsigned, f); }
#define SB() __builtin_amdgcn_sched_barrier(0)
#define BAR() asm volatile("s_waitcnt lgkmcnt(0)\n\ts_barrier" ::: "memory")
__device__ __forceinline__ unsigned rtc32_() { return (unsigned)__builtin_amdgcn_s_memrealtime(); }
__device__ __forceinline__ unsigned clk32() {
  unsigned long long t;
  asm volatile("s_memtime %0\n\ts_waitcnt lgkmcnt(0)" : "=s"(t)::"memory");
  return (unsigned)t;
}

__global__ void __launch_bounds__(256) k_init(unsigned char* base, const u16* mem0) {
  __amdgpu_buffer_rsrc_t rs = __builtin_amdgcn_make_buffer_rsrc(base, 0, (int)COMM_BYTES, 0x00020000);
  const int b = blockIdx.x, u = b * 4096 + threadIdx.x * 16;
  v4u v = {SENT, SENT, SENT, SENT};
  if (b < 8) {
    const int lo = ((u >> 14) << 13) | (u & 8191);
    const int r = lo >> 7, rp = (lo >> 4) & 7;
    v = *(const GAS v4u*)(mem0 + (4 * (r & 1) + 2 * (rp >> 2) + (rp & 1)) * DM + 16 * (r >> 1) + 8 * ((rp >> 1) & 1));
    v = v4u{min(v.x, SENT - 1u), min(v.y, SENT - 1u), min(v.z, SENT - 1u), min(v.w, SENT - 1u)};
  }
  __builtin_amdgcn_raw_buffer_store_b128(v, rs, b < 12 ? 3 * MEM_SLOT + u : 65536 + u - 12 * 4096, 0, AUX_SC1);
  if (b == 13 || b == 14) __hip_atomic_store((gu32*)(base + CTL_OFF) + CTL_CLAIM + (b - 13) * 256 + threadIdx.x, 0u, RLX_AGENT);
  if (b == 15 && threadIdx.x < CTL_WORDS / 64)
    __hip_atomic_store((gu32*)(base + CTL_OFF) + threadIdx.x * 64, threadIdx.x == 0 ? 2u : 0u, RLX_AGENT);
}

struct ChainArgs {
  const u16* Wq; const u16* Wk; const u16* Wv; const u16* Wo;
  unsigned char* base;
};
constexpr int L_RED = 0;
constexpr int L_SCHED = 13824;
constexpr int L_KVF = 14336;
constexpr int SEG_B = 20480;
constexpr int L_KX = L_KVF + 4096, L_VX = L_KX + 8192, L_QF = L_KX + 16384;
constexpr int L_OX = L_KX + 4 * SEG_B;
constexpr int L_END = L_OX + 2 * 2304;
constexpr int L_WST = 0;
constexpr int CHAIN_LDS = 96 * 1024;

__device__ __forceinline__ bool spin_fail(unsigned& spins, gu32* ctl, bool& dead) {
  if (dead) return true;
  ++spins;
  if (spins > SPIN_CAP) { __hip_atomic_store(ctl + CTL_ABORT, 1u, RLX_AGENT); dead = true; return true; }
  if ((spins & 63u) == 0u && __builtin_amdgcn_readfirstlane(__hip_atomic_load(ctl + CTL_ABORT, RLX_AGENT))) { dead = true; return true; }
  return false;
}

#define LATE 24
#define PEN 100
#define CALREP 8
#define LATEW 3
#define DEC 3
#define LATE1 24
#define WU 24
#define XA2 1
#define XA1 2
#define XMAX 8
#define START 140
#define ESC 8
#define ATT 2
#define EPS1 14
#define EPS2 5
#define EPS3 16
#define SLIP 24
#define SAFE 4
#define PFLAG 10
#define D1_0 43
#define D2_0 88
#define D3_0 88

#define STAMP(k)
#define STAMPX(k) do { if (s >= 32 && s < 40) { SB(); unsigned c_ = clk32(); SB(); if (lane == 0) stamp[((s - 32) * 4 + w) * 16 + (k)] = c_; } } while (0)
#define STAMP2(k)
#define STAMP3(k)
#define STAMP4(k)

template <int CT, int K2> __device__ __forceinline__ void wo_def(v8u& d, const v8u& v);
template <int CT, int KK> __device__ __forceinline__ void wo_mf(f32x4& d, const v4u& a, const v8u (&wo8)[4][2]);
template <int CT, int K2> __device__ __forceinline__ void wo_smf(f32x4& d, const v4u& a, const v8u (&wo8)[4][2], int idx);
template <> __device__ __forceinline__ void wo_def<0, 0>(v8u& d, const v8u& v) { asm volatile("" : "={a[192:199]}"(d) : "0"(v)); }
template <> __device__ __forceinline__ void wo_smf<0, 0>(f32x4& d, const v4u& a, const v8u (&wo8)[4][2], int idx) {
  asm volatile("v_smfmac_f32_16x16x64_bf16 %0, %1, %2, %3" : "+v"(d) : "v"(a), "{a[192:199]}"(wo8[0][0]), "v"(idx));
}
template <> __device__ __forceinline__ void wo_mf<0, 0>(f32x4& d, const v4u& a, const v8u (&wo8)[4][2]) {
  asm volatile("v_mfma_f32_16x16x32_bf16 %0, %1, a[192:195], 0" : "=&v"(d) : "v"(a), "{a[192:199]}"(wo8[0][0]));
}
template <> __device__ __forceinline__ void wo_mf<0, 1>(f32x4& d, const v4u& a, const v8u (&wo8)[4][2]) {
  asm volatile("v_mfma_f32_16x16x32_bf16 %0, %1, a[196:199], %0" : "+v"(d) : "v"(a), "{a[192:199]}"(wo8[0][0]));
}
template <> __device__ __forceinline__ void wo_def<0, 1>(v8u& d, const v8u& v) { asm volatile("" : "={a[200:207]}"(d) : "0"(v)); }
template <> __device__ __forceinline__ void wo_smf<0, 1>(f32x4& d, const v4u& a, const v8u (&wo8)[4][2], int idx) {
  asm volatile("v_smfmac_f32_16x16x64_bf16 %0, %1, %2, %3" : "+v"(d) : "v"(a), "{a[200:207]}"(wo8[0][1]), "v"(idx));
}
template <> __device__ __forceinline__ void wo_mf<0, 2>(f32x4& d, const v4u& a, const v8u (&wo8)[4][2]) {
  asm volatile("v_mfma_f32_16x16x32_bf16 %0, %1, a[200:203], %0" : "+v"(d) : "v"(a), "{a[200:207]}"(wo8[0][1]));
}
template <> __device__ __forceinline__ void wo_mf<0, 3>(f32x4& d, const v4u& a, const v8u (&wo8)[4][2]) {
  asm volatile("v_mfma_f32_16x16x32_bf16 %0, %1, a[204:207], %0" : "+v"(d) : "v"(a), "{a[200:207]}"(wo8[0][1]));
}
template <> __device__ __forceinline__ void wo_def<1, 0>(v8u& d, const v8u& v) { asm volatile("" : "={a[208:215]}"(d) : "0"(v)); }
template <> __device__ __forceinline__ void wo_smf<1, 0>(f32x4& d, const v4u& a, const v8u (&wo8)[4][2], int idx) {
  asm volatile("v_smfmac_f32_16x16x64_bf16 %0, %1, %2, %3" : "+v"(d) : "v"(a), "{a[208:215]}"(wo8[1][0]), "v"(idx));
}
template <> __device__ __forceinline__ void wo_mf<1, 0>(f32x4& d, const v4u& a, const v8u (&wo8)[4][2]) {
  asm volatile("v_mfma_f32_16x16x32_bf16 %0, %1, a[208:211], 0" : "=&v"(d) : "v"(a), "{a[208:215]}"(wo8[1][0]));
}
template <> __device__ __forceinline__ void wo_mf<1, 1>(f32x4& d, const v4u& a, const v8u (&wo8)[4][2]) {
  asm volatile("v_mfma_f32_16x16x32_bf16 %0, %1, a[212:215], %0" : "+v"(d) : "v"(a), "{a[208:215]}"(wo8[1][0]));
}
template <> __device__ __forceinline__ void wo_def<1, 1>(v8u& d, const v8u& v) { asm volatile("" : "={a[216:223]}"(d) : "0"(v)); }
template <> __device__ __forceinline__ void wo_smf<1, 1>(f32x4& d, const v4u& a, const v8u (&wo8)[4][2], int idx) {
  asm volatile("v_smfmac_f32_16x16x64_bf16 %0, %1, %2, %3" : "+v"(d) : "v"(a), "{a[216:223]}"(wo8[1][1]), "v"(idx));
}
template <> __device__ __forceinline__ void wo_mf<1, 2>(f32x4& d, const v4u& a, const v8u (&wo8)[4][2]) {
  asm volatile("v_mfma_f32_16x16x32_bf16 %0, %1, a[216:219], %0" : "+v"(d) : "v"(a), "{a[216:223]}"(wo8[1][1]));
}
template <> __device__ __forceinline__ void wo_mf<1, 3>(f32x4& d, const v4u& a, const v8u (&wo8)[4][2]) {
  asm volatile("v_mfma_f32_16x16x32_bf16 %0, %1, a[220:223], %0" : "+v"(d) : "v"(a), "{a[216:223]}"(wo8[1][1]));
}
template <> __device__ __forceinline__ void wo_def<2, 0>(v8u& d, const v8u& v) { asm volatile("" : "={a[224:231]}"(d) : "0"(v)); }
template <> __device__ __forceinline__ void wo_smf<2, 0>(f32x4& d, const v4u& a, const v8u (&wo8)[4][2], int idx) {
  asm volatile("v_smfmac_f32_16x16x64_bf16 %0, %1, %2, %3" : "+v"(d) : "v"(a), "{a[224:231]}"(wo8[2][0]), "v"(idx));
}
template <> __device__ __forceinline__ void wo_mf<2, 0>(f32x4& d, const v4u& a, const v8u (&wo8)[4][2]) {
  asm volatile("v_mfma_f32_16x16x32_bf16 %0, %1, a[224:227], 0" : "=&v"(d) : "v"(a), "{a[224:231]}"(wo8[2][0]));
}
template <> __device__ __forceinline__ void wo_mf<2, 1>(f32x4& d, const v4u& a, const v8u (&wo8)[4][2]) {
  asm volatile("v_mfma_f32_16x16x32_bf16 %0, %1, a[228:231], %0" : "+v"(d) : "v"(a), "{a[224:231]}"(wo8[2][0]));
}
template <> __device__ __forceinline__ void wo_def<2, 1>(v8u& d, const v8u& v) { asm volatile("" : "={a[232:239]}"(d) : "0"(v)); }
template <> __device__ __forceinline__ void wo_smf<2, 1>(f32x4& d, const v4u& a, const v8u (&wo8)[4][2], int idx) {
  asm volatile("v_smfmac_f32_16x16x64_bf16 %0, %1, %2, %3" : "+v"(d) : "v"(a), "{a[232:239]}"(wo8[2][1]), "v"(idx));
}
template <> __device__ __forceinline__ void wo_mf<2, 2>(f32x4& d, const v4u& a, const v8u (&wo8)[4][2]) {
  asm volatile("v_mfma_f32_16x16x32_bf16 %0, %1, a[232:235], %0" : "+v"(d) : "v"(a), "{a[232:239]}"(wo8[2][1]));
}
template <> __device__ __forceinline__ void wo_mf<2, 3>(f32x4& d, const v4u& a, const v8u (&wo8)[4][2]) {
  asm volatile("v_mfma_f32_16x16x32_bf16 %0, %1, a[236:239], %0" : "+v"(d) : "v"(a), "{a[232:239]}"(wo8[2][1]));
}
template <> __device__ __forceinline__ void wo_def<3, 0>(v8u& d, const v8u& v) { asm volatile("" : "={a[240:247]}"(d) : "0"(v)); }
template <> __device__ __forceinline__ void wo_smf<3, 0>(f32x4& d, const v4u& a, const v8u (&wo8)[4][2], int idx) {
  asm volatile("v_smfmac_f32_16x16x64_bf16 %0, %1, %2, %3" : "+v"(d) : "v"(a), "{a[240:247]}"(wo8[3][0]), "v"(idx));
}
template <> __device__ __forceinline__ void wo_mf<3, 0>(f32x4& d, const v4u& a, const v8u (&wo8)[4][2]) {
  asm volatile("v_mfma_f32_16x16x32_bf16 %0, %1, a[240:243], 0" : "=&v"(d) : "v"(a), "{a[240:247]}"(wo8[3][0]));
}
template <> __device__ __forceinline__ void wo_mf<3, 1>(f32x4& d, const v4u& a, const v8u (&wo8)[4][2]) {
  asm volatile("v_mfma_f32_16x16x32_bf16 %0, %1, a[244:247], %0" : "+v"(d) : "v"(a), "{a[240:247]}"(wo8[3][0]));
}
template <> __device__ __forceinline__ void wo_def<3, 1>(v8u& d, const v8u& v) { asm volatile("" : "={a[248:255]}"(d) : "0"(v)); }
template <> __device__ __forceinline__ void wo_smf<3, 1>(f32x4& d, const v4u& a, const v8u (&wo8)[4][2], int idx) {
  asm volatile("v_smfmac_f32_16x16x64_bf16 %0, %1, %2, %3" : "+v"(d) : "v"(a), "{a[248:255]}"(wo8[3][1]), "v"(idx));
}
template <> __device__ __forceinline__ void wo_mf<3, 2>(f32x4& d, const v4u& a, const v8u (&wo8)[4][2]) {
  asm volatile("v_mfma_f32_16x16x32_bf16 %0, %1, a[248:251], %0" : "+v"(d) : "v"(a), "{a[248:255]}"(wo8[3][1]));
}
template <> __device__ __forceinline__ void wo_mf<3, 3>(f32x4& d, const v4u& a, const v8u (&wo8)[4][2]) {
  asm volatile("v_mfma_f32_16x16x32_bf16 %0, %1, a[252:255], %0" : "+v"(d) : "v"(a), "{a[248:255]}"(wo8[3][1]));
}

template <int W> __device__ __forceinline__ void chain_loop(const ChainArgs& a, LAS unsigned char* L, int cid, const v8u (&wqkv)[4][6], const v8u (&wo8)[4][2]);

__device__ __forceinline__ void chain_role(const ChainArgs& a, LAS unsigned char* L, int cid) {
  const int tid = threadIdx.x, lane = tid & 63, w = __builtin_amdgcn_readfirstlane(tid >> 6);
  const int h = (cid & 3) * 8 + (cid >> 4), g = (cid >> 2) & 3;
  const int l15 = lane & 15, lg = lane >> 4;
  if (cid == 0 && tid == 64) {
    unsigned lat[2] = {~0u, ~0u};
    for (int rep = 0; rep < CALREP; ++rep)
      for (int b = 0; b < 2; ++b) {
        const unsigned* p = (const unsigned*)(a.base + 65536 + b * 8192 + rep * 640);
        unsigned v;
        const unsigned t0 = clk32();
        asm volatile("global_load_dword %0, %1, off sc1\n\ts_waitcnt vmcnt(0)" : "=v"(v) : "v"(p) : "memory");
        lat[b] = min(lat[b], clk32() - t0);
      }
    __hip_atomic_store((gu32*)(a.base + CTL_OFF) + CTL_POL, lat[1] < lat[0] ? 1u : 0u, RLX_AGENT);
  }
  v8u wqkv[4][6];
  v8u wo8[4][2];
  {
    LAS unsigned char* st = L + L_WST + w * 16384;
    const int q4 = l15 >> 2, p4 = lane & 3;
    v4u ld[3][16];
    auto issue = [&](int m, v4u(&d)[16]) {
      if (m < 3) {
        const u16* W = (m == 0 ? a.Wq : m == 1 ? a.Wk : a.Wv) + (size_t)(256 * w + (lane >> 2)) * 4096 + h * 128 + 32 * g + 8 * (lane & 3);
#pragma unroll
        for (int i = 0; i < 16; ++i) d[i] = *(const GAS v4u*)(W + (size_t)i * 16 * 4096);
      } else {
        const u16* W = a.Wo + (size_t)(h * 128 + (lane >> 3)) * 1024 + 256 * g + 64 * w + 8 * (lane & 7);
#pragma unroll
        for (int i = 0; i < 16; ++i) d[i] = *(const GAS v4u*)(W + (size_t)i * 8 * 1024);
      }
      SB();
    };
    issue(0, ld[0]);
    issue(1, ld[1]);
    issue(2, ld[2]);
#pragma unroll
    for (int m = 0; m < 4; ++m) {
      v4u(&d)[16] = ld[m % 3];
      if (m < 3) {
#pragma unroll
        for (int i = 0; i < 16; ++i) {
          v2u ev = {__builtin_amdgcn_perm(d[i].y, d[i].x, 0x05040100u), __builtin_amdgcn_perm(d[i].w, d[i].z, 0x05040100u)};
          v2u od = {__builtin_amdgcn_perm(d[i].y, d[i].x, 0x07060302u), __builtin_amdgcn_perm(d[i].w, d[i].z, 0x07060302u)};
          LAS unsigned char* p = st + (16 * i + (lane >> 2)) * 64 + (lane & 3) * 8;
          *(LAS v2u*)p = ev;
          *(LAS v2u*)(p + 32) = od;
        }
      } else {
#pragma unroll
        for (int i = 0; i < 16; ++i) *(LAS v4u*)(st + i * 1024 + lane * 16) = d[i];
      }
      SB();
      if (m == 0) issue(3, d);
      asm volatile("s_waitcnt lgkmcnt(0)" ::: "memory");
      if (m < 3) {
        LAS unsigned char* rb = st + (16 * (lg >> 1) + 4 * (lg & 1) + (q4 & 1) + 8 * (q4 >> 1)) * 64 + p4 * 8;
#pragma unroll
        for (int kk = 0; kk < 4; ++kk)
#pragma unroll
          for (int T = 0; T < 2; ++T) {
            v2u r0 = trread(rb + (64 * kk) * 64 + T * 32), r1 = trread(rb + (64 * kk + 2) * 64 + T * 32);
            v2u r2 = trread(rb + (64 * kk + 32) * 64 + T * 32), r3 = trread(rb + (64 * kk + 34) * 64 + T * 32);
            v8u v = {r0.x, r0.y, r1.x, r1.y, r2.x, r2.y, r3.x, r3.y};
            asm volatile("" : "=a"(wqkv[kk][m * 2 + T]) : "0"(v));
          }
      } else {
        LAS unsigned char* rb = st + (8 * lg + q4) * 128 + p4 * 16;
        auto rd = [&](int ct, int k2) {
          LAS unsigned char* q = rb + 64 * k2 * 128 + (ct >> 1) * 64 + (ct & 1) * 8;
          v2u r0 = trread(q), r1 = trread(q + 4 * 128), r2 = trread(q + 32 * 128), r3 = trread(q + 36 * 128);
          return v8u{r0.x, r0.y, r1.x, r1.y, r2.x, r2.y, r3.x, r3.y};
        };
        wo_def<0, 0>(wo8[0][0], rd(0, 0)); wo_def<0, 1>(wo8[0][1], rd(0, 1)); wo_def<1, 0>(wo8[1][0], rd(1, 0)); wo_def<1, 1>(wo8[1][1], rd(1, 1));
        wo_def<2, 0>(wo8[2][0], rd(2, 0)); wo_def<2, 1>(wo8[2][1], rd(2, 1)); wo_def<3, 0>(wo8[3][0], rd(3, 0)); wo_def<3, 1>(wo8[3][1], rd(3, 1));
      }
      asm volatile("s_waitcnt lgkmcnt(0)" ::: "memory");
    }
  }
  if (w == 0) chain_loop<0>(a, L, cid, wqkv, wo8);
  else if (w == 1) chain_loop<1>(a, L, cid, wqkv, wo8);
  else if (w == 2) chain_loop<2>(a, L, cid, wqkv, wo8);
  else chain_loop<3>(a, L, cid, wqkv, wo8);
}

__device__ __forceinline__ float fmx(float a, float b) { return __builtin_elementwise_maximum(a, b); }
__device__ __forceinline__ float fadd(float a, float b) { float r; asm("v_add_f32 %0, %1, %2" : "=v"(r) : "v"(a), "v"(b)); return r; }
#define LD16(rs, vo_, imm, so) __builtin_amdgcn_raw_buffer_load_b128(rs, (vo_) + (imm), so, AUX_SC1)

__device__ __forceinline__ unsigned rtc32() {
  unsigned long long t;
  asm volatile("s_memrealtime %0\n\ts_waitcnt lgkmcnt(0)" : "=s"(t)::"memory");
  return (unsigned)t;
}
__device__ __forceinline__ bool wait_sched(unsigned T, unsigned lt) {
  unsigned now = rtc32();
  const bool late = (int)(now - T) > SLIP;
  if (late) T = now + lt;
  for (int i = 0; i < 4096 && (int)(now - T) < 0; ++i) now = rtc32();
  return late;
}
__device__ __forceinline__ unsigned wave_umax(unsigned v) {
  v = max(v, (unsigned)__builtin_amdgcn_update_dpp(0, (int)v, 0x111, 0xf, 0xf, true));
  v = max(v, (unsigned)__builtin_amdgcn_update_dpp(0, (int)v, 0x112, 0xf, 0xf, true));
  v = max(v, (unsigned)__builtin_amdgcn_update_dpp(0, (int)v, 0x114, 0xf, 0xf, true));
  v = max(v, (unsigned)__builtin_amdgcn_update_dpp(0, (int)v, 0x118, 0xf, 0xf, true));
  return max(max(__builtin_amdgcn_readlane(v, 15), __builtin_amdgcn_readlane(v, 31)), max(__builtin_amdgcn_readlane(v, 47), __builtin_amdgcn_readlane(v, 63)));
}
__device__ __forceinline__ int imax(int a, int b) { return a > b ? a : b; }
__device__ __forceinline__ int imin(int a, int b) { return a < b ? a : b; }

__device__ __forceinline__ int track(int d, int m) { return imin(imax(m, d - DEC), d + ATT); }

template <int W>
__device__ __forceinline__ void chain_loop(const ChainArgs& a, LAS unsigned char* L, int cid, const v8u (&wqkv)[4][6], const v8u (&wo8)[4][2]) {
  const int tid = threadIdx.x, lane = tid & 63;
  constexpr int w = W;
  const int h = (cid & 3) * 8 + (cid >> 4), g = (cid >> 2) & 3, cu = h * 4 + g;
  const int l15 = lane & 15, lg = lane >> 4;
  gu32* ctl = (gu32*)(a.base + CTL_OFF);
  bool dead = false;
  unsigned pol;
  for (unsigned spins = 0; (pol = __builtin_amdgcn_readfirstlane(__hip_atomic_load(ctl + CTL_POL, RLX_AGENT))) > 1u;) {
    __builtin_amdgcn_s_sleep(8);
    if (spin_fail(spins, ctl, dead)) break;
  }
  pol &= 1u;
  __amdgpu_buffer_rsrc_t rs = __builtin_amdgcn_make_buffer_rsrc(a.base, 0, (int)COMM_BYTES, 0x00020000);
  __amdgpu_buffer_rsrc_t rx = __builtin_amdgcn_make_buffer_rsrc(a.base + XQKV_OFF, 0, SEQ * LDX * 4, 0x00020000);
  const int so_w0 = (int)(pol << 13);
  auto so_part = [&](int slot) { return ((2 + slot) << 20) + (int)(((pol ^ 1u ^ (unsigned)(slot >> 1)) & 1u) << 13); };
  const int s1_half = (l15 >> 1) & 1;
  int a_mem = vo(w * 4096 + lane * 16);
  int sidx = s1_half ? 0xEEEE : 0x4444;
  const int pub_u = (w == 0 ? lg * 128 : 512 + lg * 256 + (w == 2 ? 128 : 0)) + l15 * 8;
  const int a_pub = vo(cu * 1536 + pub_u);
  int a_g2[3], l_g2[3], l_g2p[3];
#pragma unroll
  for (int i = 0; i < 3; ++i) {
    const int o = (w & 1) * 3072 + i * 1024 + lane * 16, kk = o / 1536, u = o % 1536;
    a_g2[i] = vo(h * 6144 + o);
    l_g2[i] = u < 512 ? (L_QF + kk * 1024 + (u >> 7) * 256 + (u & 127)) : (L_KVF + kk * 1024 + u - 512);
    l_g2p[i] = u < 512 ? SEG_B : 0;
  }
  const int m_tok = 2 * (l15 >> 2) + (l15 & 1);
  const int l_km = L_KVF + lg * 256 + m_tok * 16;
  const int l_vm = L_KVF + (lg >> 1) * 1024 + (lg & 1) * 512 + (8 + m_tok) * 16 + s1_half * 4;
  const unsigned l_red0 = (unsigned)(size_t)L + L_RED + w * 3456 + lg * 288 + l15 * 8, l_red1 = l_red0 + 1152;
  const int a_ps = vo((32 * g + 8 * w + 4 * (l15 >> 3) + 2 * (lg >> 1) + ((l15 >> 2) & 1)) * 4096 + h * 128 + (lg & 1) * 64 + (l15 & 3) * 16);
  int a_pg = vo(cu * 4096 + lane * 16);
  const int a_ms = (lane < 32 && l15 < 8) ? vo(cu * 128 + (4 * ((l15 >> 1) & 1) + 2 * (l15 >> 2) + (l15 & 1)) * 16 + lg * 8) : 0x40000000;
  v4u sel;
#pragma unroll
  for (int j = 0; j < 4; ++j) sel[j] = l15 == 2 * j ? 0x00003F80u : l15 == 2 * j + 1 ? 0x3F800000u : 0u;
  PIN1(sel);
  const int a_mr = lane < 8 ? vo(cu * 128 + lane * 16) : 0x40000000;
  const int a_qr0 = vo(cu * 1536 + lane * 16), a_qr1 = lane < 32 ? vo(cu * 1536 + 1024 + lane * 16) : 0x40000000;
  const int a_ts = lane == 0 ? vo(16384 + cu * 16) : 0x40000000;
  const int a_tg = vo(16384 + lane * 16);
  const int x_far = far_off(pol, (int)(XQKV_OFF >> 21) + (h >> 1));
  const int x_kv = (((w == 2 ? 32 : 64) + h) << 20) + x_far + lg * 256 + l15 * 16;
  const int x_q = (h << 20) + x_far + (8 * g + 4 * (w & 1) + lg) * 256 + l15 * 16;
  const int l_q = L_QF + l15 * 256 + (8 + 4 * (w & 1) + lg) * 16;
  const int a_ox = (h << 20) + x_far + (8 * g + lg) * 256 + l15 * 16;
  f32x4 msk[3];
#pragma unroll
  for (int r = 0; r < 4; ++r) {
    const int qi = 8 * g + l15 - 8;
    msk[0][r] = (l15 < 8 || 4 * lg + r <= qi) ? 0.f : -1e30f;
    msk[1][r] = (l15 < 8 || 16 + 4 * lg + r <= qi) ? 0.f : -1e30f;
    msk[2][r] = r < 2 ? 0.f : -1e30f;
  }
  v4u ones = {0x3F803F80u, 0x3F803F80u, 0x3F803F80u, 0x3F803F80u};
  v4u sentv = {SENT, SENT, SENT, SENT};
  v4u bm[4];
#pragma unroll
  for (int ct = 0; ct < 4; ++ct) bm[ct] = v4u{0u, 0u, 0u, 0u};
  PIN2(ones, sentv);
  PIN2(bm[0], bm[1]);
  PIN2(bm[2], bm[3]);
  PIN2(msk[0], msk[1]);
  PIN2(msk[2], sidx);
  LAS unsigned* sched = (LAS unsigned*)(L + L_SCHED);

  auto ox_store = [&](int s) {
#pragma unroll
    for (int j = 0; j < 2; ++j) {
      const v4u v = *(LAS v4u*)(L + L_OX + (s & 1) * 2304 + (4 * j + lg) * 272 + l15 * 16);
      __builtin_amdgcn_raw_buffer_store_b128(v, rx, a_ox, s * 16384 + j * 1024, AUX_SC1);
    }
    SB();
  };
#define ZTILE(ct) \
    wo_mf<ct, 0>(zc[(ct) & 1][0], vf[0][0], wo8); wo_mf<ct, 0>(zc[(ct) & 1][1], vf[1][0], wo8); \
    wo_mf<ct, 1>(zc[(ct) & 1][0], vf[0][1], wo8); wo_mf<ct, 1>(zc[(ct) & 1][1], vf[1][1], wo8); \
    wo_mf<ct, 2>(zc[(ct) & 1][0], vf[0][2], wo8); wo_mf<ct, 2>(zc[(ct) & 1][1], vf[1][2], wo8); \
    wo_mf<ct, 3>(zc[(ct) & 1][0], vf[0][3], wo8); wo_mf<ct, 3>(zc[(ct) & 1][1], vf[1][3], wo8)
#define ZCVT(ct) \
    PIN2(zc[(ct) & 1][0], zc[(ct) & 1][1]); \
    zx[ct] = v4u{cvt_pk(zc[(ct) & 1][0][0], zc[(ct) & 1][0][1]), cvt_pk(zc[(ct) & 1][0][2], zc[(ct) & 1][0][3]), \
                 cvt_pk(zc[(ct) & 1][1][0], zc[(ct) & 1][1][1]), cvt_pk(zc[(ct) & 1][1][2], zc[(ct) & 1][1][3])}; \
    PIN1(zx[ct]); SB()
  v4u zx[4];
  const int fo0 = lg * 256 + ((l15 ^ lg) * 16), fo1 = 1024 + lg * 256 + ((l15 ^ (4 + lg)) * 16);
  auto z_pair = [&](auto CT, int seg) {
    constexpr int ct = decltype(CT)::value;
    LAS unsigned char* vb = L + L_VX + (seg & 3) * SEG_B;
    v4u vf[2][4];
    f32x4 zc[2][2];
#pragma unroll
    for (int kk = 0; kk < 4; ++kk)
#pragma unroll
      for (int kt = 0; kt < 2; ++kt) vf[kt][kk] = *(LAS v4u*)(vb + ((kk & 1) ? fo1 : fo0) + kt * 4096 + (kk >> 1) * 2048);
    SB();
    ZTILE(ct);
    ZTILE(ct + 1);
    ZCVT(ct);
    NOPS(4);
    ZCVT(ct + 1);
  };
  v4u pf[9];
  auto pf_issue = [&](int s) {
    const int so = s * 16384;
#pragma unroll
    for (int i = 0; i < 8; ++i) pf[i] = __builtin_amdgcn_raw_buffer_load_b128(rx, x_kv, so + i * 1024, AUX_SC1);
    pf[8] = __builtin_amdgcn_raw_buffer_load_b128(rx, x_q, so, AUX_SC1);
    SB();
  };
  auto pf_write = [&](int s) {
    LAS unsigned char* d = L + (w == 2 ? L_KX : L_VX) + (s & 3) * SEG_B + l15 * 256;
#pragma unroll
    for (int i = 0; i < 8; ++i) {
      const int row = 4 * i + lg;
      *(LAS v4u*)(d + (row >> 4) * 4096 + (((row & 15) ^ (l15 & 7)) * 16)) = pf[i];
    }
    *(LAS v4u*)(L + l_q + (s & 3) * SEG_B) = pf[8];
  };
  BAR();
  auto rb_wait = [&](unsigned have, int rb) {
    for (unsigned spins = 0; have < 96u; have = __builtin_amdgcn_readfirstlane(__hip_atomic_load(ctl + CTL_RB + 64 * rb, RLX_AGENT))) {
      __builtin_amdgcn_s_sleep(8);
      if (spin_fail(spins, ctl, dead)) break;
    }
    __builtin_amdgcn_fence(__ATOMIC_ACQUIRE, "wavefront");
  };
  unsigned rbf = 96u;
  if (w >= 2) {
    rb_wait(0u, 0); pf_issue(0); pf_write(0); pf_issue(1);
  } else {
#pragma unroll
    for (int sl = 0; sl < 3; ++sl) {
      if (w == 0) {
#pragma unroll
        for (int k = 0; k < 4; ++k) __builtin_amdgcn_raw_buffer_store_b128(sentv, rs, a_pg + k * 1024, so_part(sl), AUX_SC1);
      } else {
        __builtin_amdgcn_raw_buffer_store_b128(sentv, rs, a_qr0, so_w0 + QKV_BASE + sl * QKV_SLOT, AUX_SC1);
        __builtin_amdgcn_raw_buffer_store_b128(sentv, rs, a_qr1, so_w0 + QKV_BASE + sl * QKV_SLOT, AUX_SC1);
        __builtin_amdgcn_raw_buffer_store_b128(sentv, rs, a_mr, so_w0 + sl * MEM_SLOT, AUX_SC1);
        __builtin_amdgcn_raw_buffer_store_b128(sentv, rs, a_ts, so_w0 + sl * MEM_SLOT, AUX_SC1);
      }
    }
    asm volatile("s_waitcnt vmcnt(0)" ::: "memory");
  }
  const unsigned tb = rtc32() - (1u << 24);
  int x1 = EPS1, x2 = EPS2;
  int esc = 0, c1 = 0, p1 = 0, p2 = 0, p3 = 0, d1 = D1_0, d2 = D2_0, d3 = D3_0;
  bool pok = false;
  if (w == 3 && lane == 0) { sched[4] = 0u; sched[5] = 0u; sched[11] = ~0u; }
  BAR();
  if (w == 1) {
    const unsigned t = min(rtc32(), SENT - 1u);
    __builtin_amdgcn_raw_buffer_store_b128(v4u{t, t, t, 0u}, rs, a_ts, so_w0 + 3 * MEM_SLOT, AUX_SC1);
    v4u t0, t1;
    for (unsigned spins = 0;;) {
      t0 = LD16(rs, a_tg, 0, so_w0 + 3 * MEM_SLOT);
      t1 = LD16(rs, a_tg, 0, so_w0 + 3 * MEM_SLOT + 1024);
      if (!__any(max(t0.z, t1.z) == SENT)) break;
      if (spin_fail(spins, ctl, dead)) break;
    }
    c1 = (int)wave_umax(max(t0.z - tb, t1.z - tb)) + START;
    if (lane == 0) *(LAS v4u*)sched = v4u{tb + (unsigned)c1, 0u, 0u, ~0u};
  }
  z_pair(std::integral_constant<int, 0>(), 0);
  if (w >= 2) z_pair(std::integral_constant<int, 2>(), 0);
  BAR();
  unsigned I1 = __builtin_amdgcn_readfirstlane(sched[0]), I2 = I1, I3 = I1, I2n = I1 + D2_0 + EPS2 + WU, I3n = I2n + D3_0 + EPS3 + WU;

#define CNT(k)
  for (int s = 0; s < NSEG; ++s) {
    const int sb = (s & 3) * SEG_B;
    STAMP(0);
    v4u kxf[2][4], mf[4];
    const int so_m = so_w0 + ((s + 3) & 3) * MEM_SLOT;
    if (w != 0) {
      for (int i = 0; i < (1 << 24); ++i) {
        unsigned f;
        asm volatile("ds_read_b32 %0, %1\n\ts_waitcnt lgkmcnt(0)" : "=v"(f) : "v"((unsigned)(size_t)(sched + 4)) : "memory");
        if (__builtin_amdgcn_readfirstlane(f) == (unsigned)s) break;
      }
    }
    bool off = wait_sched(I1, LATE1);
    STAMP4(0);
    PIN1(a_mem);
#pragma unroll
    for (int kk = 0; kk < 4; ++kk) mf[kk] = LD16(rs, a_mem, kk * 1024, so_m);
    SB();
    if (w >= 2) {
      pf_write(s + 1);
      rb_wait(__builtin_amdgcn_readfirstlane(rbf), min(s + 2, NSEG - 1) >> 2);
      if (w == 2) {
        PIN1(a_pg);
#pragma unroll
        for (int k = 0; k < 4; ++k) __builtin_amdgcn_raw_buffer_store_b128(sentv, rs, a_pg + k * 1024, so_part((s + 3) & 3), AUX_SC1);
      }
    }
    if (w < 2) z_pair(std::integral_constant<int, 2>(), s);
    f32x4 acc[6];
#pragma unroll
    for (int j = 0; j < 6; ++j) { acc[j] = f32x4{0.f, 0.f, 0.f, 0.f}; PIN1(acc[j]); }
    NOPS(2);
    SB();
    STAMP(1);
    STAMP4(1);
    bool bad = false;
    {
      unsigned mx = 0;
#pragma unroll
      for (int kk = 0; kk < 4; ++kk) {
        SMF_A(acc[0], mf[kk], wqkv[kk][0], sidx);
        mx = umax4(mx, mf[kk]);
        PIN1(mx);
        SB();
        SMF_A(acc[1], mf[kk], wqkv[kk][1], sidx);
        if (kk == 3) { bad = __any(mx == SENT); SB(); }
#pragma unroll
        for (int j = 2; j < 6; ++j) SMF_A(acc[j], mf[kk], wqkv[kk][j], sidx);
        SB();
        if (kk == 0) STAMP4(2);
      }
    }
    if (__builtin_expect(bad, 0)) {
      v4u m2f[4] = {mf[0], mf[1], mf[2], mf[3]};
      for (unsigned spins = 0;;) {
        CNT(0);
        if (spin_fail(spins, ctl, dead)) break;
        bool b[4];
#pragma unroll
        for (int kk = 0; kk < 4; ++kk) b[kk] = __any(umax4(0u, m2f[kk]) == SENT);
        if (!(b[0] | b[1] | b[2] | b[3])) break;
        v4u t[4];
#pragma unroll
        for (int kk = 0; kk < 4; ++kk) t[kk] = LD16(rs, b[kk] ? a_mem + kk * 1024 : 0x40000000, 0, so_m);
        PIN2(t[0], t[1]);
        PIN2(t[2], t[3]);
#pragma unroll
        for (int kk = 0; kk < 4; ++kk) m2f[kk] = b[kk] ? t[kk] : m2f[kk];
      }
      off = true;
      f32x4 r[6];
#pragma unroll
      for (int j = 0; j < 6; ++j) { r[j] = f32x4{0.f, 0.f, 0.f, 0.f}; PIN1(r[j]); }
      NOPS(2);
      SB();
#pragma unroll
      for (int kk = 0; kk < 4; ++kk)
#pragma unroll
        for (int j = 0; j < 6; ++j) SMF_A(r[j], m2f[kk], wqkv[kk][j], sidx);
      NOPS(8);
#pragma unroll
      for (int j = 0; j < 6; ++j) { PIN1(r[j]); acc[j] = r[j]; }
      SB();
    }
    STAMP(2);
    STAMP4(3);
    {
      NOPS(3);
#pragma unroll
      for (int m = 0; m < 3; ++m) {
        f32x2 u, v;
        asm volatile("v_add_f32 %0, %4, %5\n\tv_add_f32 %1, %6, %7\n\tv_add_f32 %2, %8, %9\n\tv_add_f32 %3, %10, %11"
                     : "=&v"(u.x), "=&v"(u.y), "=&v"(v.x), "=&v"(v.y)
                     : "v"(acc[2 * m][0]), "v"(acc[2 * m][2]), "v"(acc[2 * m + 1][0]), "v"(acc[2 * m + 1][2]), "v"(acc[2 * m][1]), "v"(acc[2 * m][3]),
                       "v"(acc[2 * m + 1][1]), "v"(acc[2 * m + 1][3]));
        asm volatile("ds_write2_b64 %0, %1, %2 offset0:%3 offset1:%4" ::"v"(m < 1 ? l_red0 : l_red1), "v"(u), "v"(v), "i"(m == 2 ? 144 : 0), "i"(m == 2 ? 162 : 18)
                     : "memory");
      }
    }
    if (__builtin_expect(off, 0)) sched[5] = (unsigned)(s + 1);
    v4u tsv[2];
    STAMP4(4);
    BAR();
    STAMP4(5);
    const int so_q = so_w0 + QKV_BASE + (s & 3) * QKV_SLOT;
    unsigned offw = 0;
    int so_p = so_part(s & 3);
    asm volatile("" : "+s"(so_p));
    bool offs = false;
    if (w < 3) {
      LAS f32x4* rr = (LAS f32x4*)(L + L_RED + w * 1152 + (l15 >> 1) * 144 + lg * 32 + (lane & 1) * 16);
      f32x4 x0 = rr[0], x1 = rr[216], x2 = rr[432], x3 = rr[648];
      offw = sched[5];
      f32x4 x = (x0 + x1) + (x2 + x3);
      if (w == 0) x *= CEXP;
      __builtin_amdgcn_raw_buffer_store_b64(v2u{cvt_pk(x[0], x[1]), cvt_pk(x[2], x[3])}, rs, a_pub, so_q, AUX_SC1);
      SB();
    }
    STAMP(3);
    STAMP4(6);
    unsigned tq = 0;
    v4u q2[3];
    int lw[3];
    if (w == 0) tq = rtc32();
    auto plan = [&]() {
      int a1 = 0, n2 = 0, n3 = 0, n1 = 0;
      bool ok = false;
      const int wu = s < 2 ? WU >> s : 0;
      {
        for (unsigned spins = 0;;) {
          if (!__any(umax3(umax3(tsv[0].x, tsv[0].y, tsv[0].z), tsv[1].x, umax3(0u, tsv[1].y, tsv[1].z)) == SENT)) break;
          if (spin_fail(spins, ctl, dead)) break;
          tsv[0] = LD16(rs, a_tg, 0, so_m);
          tsv[1] = LD16(rs, a_tg, 0, so_m + 1024);
          PIN2(tsv[0], tsv[1]);
        }
        const int Tm = (int)wave_umax(max(tsv[0].z - tb, tsv[1].z - tb));
        ok = Tm + EPS1 - SAFE <= c1;
        const bool pok1 = pok;
        esc = ok ? 0 : imin(esc + ESC, 8 * ESC);
        a1 = ok ? c1 : imax(c1, Tm) + PEN;
        n2 = ok ? (int)(I2n - tb) : a1 + d2 + LATE;
        n3 = ok ? (int)(I3n - tb) : n2 + d3 + EPS3 + esc - ESC;
        n1 = n3 + d1 + x1 + (esc >> 1) + (wu >> 1);
        I2 = tb + (unsigned)n2; I3 = tb + (unsigned)n3; I1 = tb + (unsigned)n1;
        if (lane == 0) *(LAS v4u*)sched = v4u{I1, I2, I3, (unsigned)s};
        SB();
        const int Tq = (int)wave_umax(max(tsv[0].x - tb, tsv[1].x - tb)), Tp = (int)wave_umax(max(tsv[0].y - tb, tsv[1].y - tb));
        if (pok) d2 = track(d2, Tq - p1);
        if (pok && Tq + EPS2 - SAFE <= p2) d3 = track(d3, Tp - p2);
        if (s && Tp + EPS3 - SAFE <= p3) d1 = track(d1, Tm - p3);
        if (pok1 && !ok && s > 2) {
          if (Tq + EPS2 - SAFE > p2) { x2 = imin(x2 + XA2, EPS2 + XMAX); x1 = imin(x1 + XA1, EPS1 + XMAX); }
        }
      }
      I2n = I1 + (unsigned)(d2 + x2 + (wu >> 1));
      I3n = I2n + (unsigned)(d3 + EPS3 + (wu >> 1));
      if (lane == 0) *(LAS v4u*)(sched + 8) = v4u{I2n, I3n, 0u, (unsigned)s};
      p1 = a1; p2 = n2; p3 = n3; c1 = n1; pok = ok;
    };
    auto ox_out = [&]() {
      if (w == 3 && s > 0) {
        if ((s & (s > 8 * NRB1 + 1 ? 3 : 7)) == 1 && s > 1 && lane == 0)
          __hip_atomic_fetch_add(ctl + CTL_OX + 64 * ((s > 8 * NRB1 + 1 ? (s >> 2) - 7 : (s >> 3) - 1) * 4 + (h >> 3)), 1u, RLX_AGENT);
        ox_store(s - 1);
      }
    };
    if (w == 3) {
      offw = sched[5];
      asm volatile("s_waitcnt vmcnt(0)" ::: "memory");
    }
    auto get4 = [&](int o) {
      v4u sc;
      do {
        asm volatile("ds_read_b128 %0, %1\n\ts_waitcnt lgkmcnt(0)" : "=v"(sc) : "v"((unsigned)(size_t)(sched + o)) : "memory");
      } while (__builtin_amdgcn_readfirstlane(sc.w) != (unsigned)s);
      return sc;
    };
    auto get_sched = [&]() {
      const v4u sc = get4(0);
      I1 = __builtin_amdgcn_readfirstlane(sc.x);
      I2 = __builtin_amdgcn_readfirstlane(sc.y);
      I3 = __builtin_amdgcn_readfirstlane(sc.z);
    };
    auto get_next = [&]() {
      get_sched();
      const v4u sc = get4(8);
      I2n = __builtin_amdgcn_readfirstlane(sc.x);
      I3n = __builtin_amdgcn_readfirstlane(sc.y);
    };
    auto k_frags = [&]() {
#pragma unroll
      for (int kt = 0; kt < 2; ++kt)
#pragma unroll
        for (int kk = 0; kk < 4; ++kk) kxf[kt][kk] = *(LAS v4u*)(L + L_KX + sb + ((kk & 1) ? fo1 : fo0) + kt * 4096 + (kk >> 1) * 2048);
      SB();
    };
    if (w < 2) {
#pragma unroll
      for (int i = 0; i < 3; ++i) { lw[i] = l_g2[i] + (s & 3) * l_g2p[i]; PIN1(lw[i]); }
      if (w == 1) {
        tsv[0] = LD16(rs, a_tg, 0, so_m);
        tsv[1] = LD16(rs, a_tg, 0, so_m + 1024);
        SB();
      }
      offs = __builtin_amdgcn_readfirstlane(offw) == (unsigned)(s + 1);
      I3 = I3n;
      wait_sched(offs ? tb : I2n, LATE);
      STAMP2(0);
#pragma unroll
      for (int i = 0; i < 3; ++i) q2[i] = LD16(rs, a_g2[i], 0, so_q);
      SB();
      k_frags();
    } else {
      k_frags();
      offs = __builtin_amdgcn_readfirstlane(offw) == (unsigned)(s + 1);
      const unsigned Tpf = offs ? rtc32() + LATE + PFLAG + (w == 3 ? 10 : 0) : I2n + PFLAG;
      for (int i = 0; i < 4096 && (int)(rtc32() - Tpf) < 0; ++i) {}
      pf_issue(min(s + 2, NSEG - 1));
      ox_out();
      rbf = __hip_atomic_load(ctl + CTL_RB + 64 * (min(s + 3, NSEG - 1) >> 2), RLX_AGENT);
      if (w == 2) {
        const int so_r = so_w0 + QKV_BASE + ((s + 3) & 3) * QKV_SLOT;
        __builtin_amdgcn_raw_buffer_store_b128(sentv, rs, a_qr0, so_r, AUX_SC1);
        __builtin_amdgcn_raw_buffer_store_b128(sentv, rs, a_qr1, so_r, AUX_SC1);
        __builtin_amdgcn_raw_buffer_store_b128(sentv, rs, a_mr, so_w0 + ((s + 2) & 3) * MEM_SLOT, AUX_SC1);
        __builtin_amdgcn_raw_buffer_store_b128(sentv, rs, a_ts, so_w0 + ((s + 1) & 3) * MEM_SLOT, AUX_SC1);
      }
      SB();
    }
    f32x4 vw[4];
#pragma unroll
    for (int ct = 0; ct < 4; ++ct) { vw[ct] = f32x4{0.f, 0.f, 0.f, 0.f}; PIN1(vw[ct]); }
    SB();
    if (w < 2) {
      for (unsigned spins = 0;;) {
        unsigned mx = 0;
#pragma unroll
        for (int i = 0; i < 3; ++i) {
          *(LAS v4u*)(L + lw[i]) = q2[i];
          mx = umax4(mx, q2[i]);
          SB();
          if (i == 0) STAMP2(1);
        }
        if (!__any(mx == SENT)) break;
        CNT(1);
        if (spin_fail(spins, ctl, dead)) break;
#pragma unroll
        for (int i = 0; i < 3; ++i) q2[i] = LD16(rs, a_g2[i], 0, so_q);
        SB();
      }
    }
    STAMP(4);
    STAMP2(2);
    BAR();
    STAMP2(3);
    v4u bx;
    float rinv;
    {
      v4u qf[4], kmf[4], vms[2];
#pragma unroll
      for (int kk = 0; kk < 4; ++kk) {
        qf[kk] = *(LAS v4u*)(L + L_QF + sb + kk * 1024 + lane * 16);
        SB();
        kmf[kk] = *(LAS v4u*)(L + l_km + kk * 1024);
        SB();
      }
#pragma unroll
      for (int k2 = 0; k2 < 2; ++k2) {
        LAS unsigned* vp = (LAS unsigned*)(L + l_vm + k2 * 2048);
        vms[k2] = v4u{vp[0], vp[2], vp[64], vp[66]};
      }
      SB();
      f32x4 s0, s1, sm;
      MF_C(s0, kxf[0][0], qf[0], msk[0]);
      MF_C(s1, kxf[1][0], qf[0], msk[1]);
      MF_C(sm, kmf[0], qf[0], msk[2]);
#pragma unroll
      for (int kk = 1; kk < 4; ++kk) {
        MF_A(s0, kxf[0][kk], qf[kk], "v");
        MF_A(s1, kxf[1][kk], qf[kk], "v");
        MF_A(sm, kmf[kk], qf[kk], "v");
      }
#define VW(i) wo_smf<(i) & 3, ((i) >> 2)>(vw[(i) & 3], vms[(i) >> 2], wo8, sidx)
#define EX(x) __builtin_amdgcn_exp2f(x)
      STAMP2(4);
      float e0[4], e1[4], em[2];
      VW(0); VW(1);
      NOPS(4);
      PIN1(s0);
      e0[0] = EX(s0[0]);
      PIN1(e0[0]);
      SB();
      VW(2);
      e0[1] = EX(s0[1]);
      SB();
      VW(3);
      e0[2] = EX(s0[2]);
      e0[3] = EX(s0[3]);
      SB();
      VW(4);
      PIN1(s1);
      e1[0] = EX(s1[0]);
      bx.x = cvt_pk(e0[0], e0[1]);
      SB();
      VW(5);
      e1[1] = EX(s1[1]);
      bx.y = cvt_pk(e0[2], e0[3]);
      SB();
      VW(6);
      e1[2] = EX(s1[2]);
      e1[3] = EX(s1[3]);
      SB();
      VW(7);
      PIN1(sm);
      em[0] = EX(sm[0]);
      bx.z = cvt_pk(e1[0], e1[1]);
      bx.w = cvt_pk(e1[2], e1[3]);
      PIN1(bx);
      SB();
      em[1] = EX(sm[1]);
      PIN2(em[0], em[1]);
      NOPS(1);
      SB();
      STAMP2(6);
      f32x4 ls, o[4];
      v4u zm;
#define FOLD(d, v) asm volatile("v_add_f32 %1, %3, %5\n\tv_add_f32 %2, %4, %6\n\tv_cvt_pk_bf16_f32 %0, %1, %2" : "=v"(d), "=&v"(f0), "=&v"(f1) : "v"(v[0]), "v"(v[1]), "v"(v[2]), "v"(v[3]))
      float f0, f1;
      MF_Z(ls, ones, bx, "v");
      asm volatile("v_cvt_pk_bf16_f32 %0, %4, %5\n\tv_cvt_pk_bf16_f32 %1, %4, %5\n\tv_cvt_pk_bf16_f32 %2, %4, %5\n\tv_cvt_pk_bf16_f32 %3, %4, %5"
                   : "=&v"(bm[0].x), "=&v"(bm[1].y), "=&v"(bm[2].z), "=&v"(bm[3].w) : "v"(em[0]), "v"(em[1]));
      MF_Z(o[0], zx[0], bx, "v");
      FOLD(zm.x, vw[0]);
      MF_A(ls, ones, bm[0], "v");
      FOLD(zm.y, vw[1]);
      MF_Z(o[1], zx[1], bx, "v");
      FOLD(zm.z, vw[2]);
      MF_Z(o[2], zx[2], bx, "v");
      FOLD(zm.w, vw[3]);
      MF_Z(o[3], zx[3], bx, "v");
      NOPS(1);
      MF_A(o[0], zm, bm[0], "v");
      MF_A(o[1], zm, bm[1], "v");
      PIN1(ls);
      rinv = __builtin_amdgcn_rcpf(ls[0]);
      const bool redo = __any(!(ls[0] > 1e-15f && ls[0] < 1e15f));
      PIN1(rinv);
      SB();
      MF_A(o[2], zm, bm[2], "v");
      MF_A(o[3], zm, bm[3], "v");
      if (__builtin_expect(redo || FORCE_SAFE, 0)) {
        float mx = fmx(fmx(fmx(fmx(s0[0], s0[1]), fmx(s0[2], s0[3])), fmx(fmx(s1[0], s1[1]), fmx(s1[2], s1[3]))), fmx(sm[0], sm[1]));
        auto r = __builtin_amdgcn_permlane16_swap(ubits(mx), ubits(mx), false, false);
        mx = fmx(fbits(r[0]), fbits(r[1]));
        auto r2 = __builtin_amdgcn_permlane32_swap(ubits(mx), ubits(mx), false, false);
        mx = fmx(fbits(r2[0]), fbits(r2[1]));
        v4u cx = {cvt_pk(EX(s0[0] - mx), EX(s0[1] - mx)), cvt_pk(EX(s0[2] - mx), EX(s0[3] - mx)), cvt_pk(EX(s1[0] - mx), EX(s1[1] - mx)),
                  cvt_pk(EX(s1[2] - mx), EX(s1[3] - mx))};
        const unsigned pm = cvt_pk(EX(sm[0] - mx), EX(sm[1] - mx));
        v4u cm[4] = {v4u{pm, 0u, 0u, 0u}, v4u{0u, pm, 0u, 0u}, v4u{0u, 0u, pm, 0u}, v4u{0u, 0u, 0u, pm}};
        PIN2(cm[0], cm[1]);
        PIN2(cm[2], cm[3]);
        PIN1(cx);
        NOPS(8);
        f32x4 l2, o2[4];
        MF_Z(l2, ones, cx, "v");
        MF_A(l2, ones, cm[0], "v");
#pragma unroll
        for (int ct = 0; ct < 4; ++ct) { MF_Z(o2[ct], zx[ct], cx, "v"); MF_A(o2[ct], zm, cm[ct], "v"); }
        NOPS(8);
        PIN2(o2[0], o2[1]);
        PIN2(o2[2], o2[3]);
        PIN1(l2);
        rinv = __builtin_amdgcn_rcpf(l2[0]);
        bx = cx;
        bm[0].x = pm;
#pragma unroll
        for (int ct = 0; ct < 4; ++ct) o[ct] = o2[ct];
        PIN2(o[0], o[1]);
        PIN2(o[2], o[3]);
        NOPS(2);
      }
      NOPS(2);
      PIN2(o[0], o[1]);
      o[0] *= rinv;
      o[1] *= rinv;
      PIN2(o[0], o[1]);
      v4u va = {cvt_pk(o[0][0], o[0][1]), cvt_pk(o[0][2], o[0][3]), cvt_pk(o[1][0], o[1][1]), cvt_pk(o[1][2], o[1][3])};
      PIN1(va);
      PIN2(o[2], o[3]);
      o[2] *= rinv;
      o[3] *= rinv;
      PIN2(o[2], o[3]);
      v4u vb = {cvt_pk(o[2][0], o[2][1]), cvt_pk(o[2][2], o[2][3]), cvt_pk(o[3][0], o[3][1]), cvt_pk(o[3][2], o[3][3])};
#pragma unroll
      for (int r = 0; r < 4; ++r) va[r] = __builtin_amdgcn_update_dpp(va[r], vb[r], 0x118, 0xf, 0xc, false);
      __builtin_amdgcn_raw_buffer_store_b128(va, rs, a_ps, so_p, AUX_SC1);
      SB();
    }
    STAMP(5);
    STAMP2(7);
    v4u pg[4];
    unsigned tp = 0;
    if (w == 0) tp = rtc32();
    if (w == 1) plan();
    auto ox_part = [&]() {
      const int q4 = l15 >> 2, p4 = lane & 3;
      f32x4 o[2];
      v4u ta[2], tb2[2];
#pragma unroll
      for (int j = 0; j < 2; ++j) {
        const int chunk = 2 * (2 * w + j) + (p4 >> 1);
        LAS unsigned char* vp = L + L_VX + sb + chunk * 256 + (((4 * lg + q4) ^ (chunk & 7)) * 16) + (p4 & 1) * 8;
        v2u t0 = trread(vp), t1 = trread(vp + 4096);
        v2u tm = trread(L + L_KVF + chunk * 256 + (8 + 2 * lg + (q4 & 1)) * 16 + (p4 & 1) * 8);
        ta[j] = v4u{t0.x, t0.y, t1.x, t1.y};
        tb2[j] = v4u{tm.x, tm.y, t0.x, t0.y};
      }
      PIN2(ta[0], tb2[0]);
      PIN2(ta[1], tb2[1]);
      NOPS(2);
      MF_Z(o[0], ta[0], bx, "v");
      MF_Z(o[1], ta[1], bx, "v");
      MF_A(o[0], tb2[0], bm[0], "v");
      MF_A(o[1], tb2[1], bm[0], "v");
      NOPS(8);
      PIN2(o[0], o[1]);
#pragma unroll
      for (int j = 0; j < 2; ++j)
        *(LAS v2u*)(L + L_OX + (s & 1) * 2304 + (l15 >= 8 ? (l15 - 8) * 272 + (32 * w + 16 * j + 4 * lg) * 2 : 2176 + lg * 8)) =
            v2u{cvt_pk(o[j][0] * rinv, o[j][1] * rinv), cvt_pk(o[j][2] * rinv, o[j][3] * rinv)};
      SB();
    };
    ox_part();
    if (w == 0) {
      if (offs) get_sched();
      wait_sched(I3, LATE);
      STAMP3(0);
      PIN1(a_pg);
#pragma unroll
      for (int k = 0; k < 4; ++k) pg[k] = LD16(rs, a_pg, k * 1024, so_p);
      SB();
    }
    z_pair(std::integral_constant<int, 0>(), s + 1);
    if (w >= 2) z_pair(std::integral_constant<int, 2>(), s + 1);
    if (w >= 2) get_next();
    if (w == 0 && s + 1 < NSEG) {
      STAMP3(1);
      f32x4 sum;
      for (unsigned spins = 0;;) {
        unsigned mx = umax4(0u, pg[0]);
        MF_Z(sum, sel, pg[0], "v");
#pragma unroll
        for (int k = 1; k < 4; ++k) {
          MF_A(sum, sel, pg[k], "v");
          mx = umax4(mx, pg[k]);
          PIN1(mx);
          SB();
          if (k == 1) STAMP3(2);
        }
        STAMP3(3);
        if (!__any(mx == SENT)) break;
        CNT(2);
        if (spin_fail(spins, ctl, dead)) break;
        PIN1(a_pg);
#pragma unroll
        for (int k = 0; k < 4; ++k) pg[k] = LD16(rs, a_pg, k * 1024, so_p);
        NOPS(4);
        SB();
      }
      const unsigned tm = rtc32();
      v4u tsw = {min(tq, SENT - 1u), min(tp, SENT - 1u), min(tm, SENT - 1u), 0u};
      PIN2(sum, tsw);
#pragma unroll
      for (int r = 0; r < 4; ++r) sum[r] += fbits(__builtin_amdgcn_update_dpp(0u, ubits(sum[r]), 0x128, 0xf, 0xf, true));
      const int so_o = so_w0 + (s & 3) * MEM_SLOT;
      __builtin_amdgcn_raw_buffer_store_b64(v2u{cvt_pk(sum[0], sum[1]), cvt_pk(sum[2], sum[3])}, rs, a_ms, so_o, AUX_SC1);
      SB();
      __builtin_amdgcn_raw_buffer_store_b128(tsw, rs, a_ts, so_o, AUX_SC1);
      sched[4] = (unsigned)(s + 1);
      SB();
      get_next();
      SB();
    }
    STAMP(6);
    STAMP3(4);
  }
  BAR();
  if (w == 3) {
    ox_store(NSEG - 1);
    asm volatile("s_waitcnt vmcnt(0)" ::: "memory");
    if (lane == 0) __hip_atomic_fetch_add(ctl + CTL_OX + 64 * (9 * 4 + (h >> 3)), 1u, RLX_AGENT);
  }
}

#define GDMA0 1
#define GDMAS 4
#define GDMAL 2
#define GBAR 28
#define GSTAMP(j)
#define SSTAMP()
#define LSTAMP(k)
struct GItem {
  const u16* A; const u16* W; u16* C;
  int lda, ldw, ldc, nk;
  int afar;
  int c16, c32, c128;
  int cwin;
  int cjump;
  int cpanel;
  float scale;
  int flag;
  int two;
};
constexpr int G_STAGE = 131072;
constexpr int GEMM_LDS = G_STAGE + 16384;

__device__ __forceinline__ f32x16 mfma32(v4u a, v4u b, f32x16 c) {
  return __builtin_amdgcn_mfma_f32_32x32x16_bf16(__builtin_bit_cast(bf16x8, a), __builtin_bit_cast(bf16x8, b), c, 0, 0, 0);
}
__device__ __forceinline__ void dma16(unsigned m0v, int voff, __amdgpu_buffer_rsrc_t rs, int soff) {
  asm volatile("s_mov_b32 m0, %0\n\ts_nop 0\n\tbuffer_load_dwordx4 %1, %2, %3 offen sc1 lds" ::"s"(m0v), "v"(voff), "s"(rs), "s"(soff) : "memory");
}
template <unsigned OFF> __device__ __forceinline__ void dma_m0(unsigned base) { asm volatile("s_add_u32 m0, %0, %1" ::"s"(base), "i"(OFF) : "memory", "scc"); }
__device__ __forceinline__ void dma_go(int voff, __amdgpu_buffer_rsrc_t rs, int soff) {
  asm volatile("buffer_load_dwordx4 %0, %1, %2 offen lds" ::"v"(voff), "s"(rs), "s"(soff) : "memory");
}
__device__ __forceinline__ void dma_go1(int voff, __amdgpu_buffer_rsrc_t rs, int soff) {
  asm volatile("buffer_load_dwordx4 %0, %1, %2 offen sc1 lds" ::"v"(voff), "s"(rs), "s"(soff) : "memory");
}
__device__ __forceinline__ __amdgpu_buffer_rsrc_t mk_rsrc(const void* p, int bytes) {
  const unsigned long long u = (unsigned long long)p;
  const unsigned lo = __builtin_amdgcn_readfirstlane((unsigned)u), hi = __builtin_amdgcn_readfirstlane((unsigned)(u >> 32));
  return __builtin_amdgcn_make_buffer_rsrc((void*)(((unsigned long long)hi << 32) | lo), 0, __builtin_amdgcn_readfirstlane(bytes), 0x00020000);
}

__device__ __forceinline__ GItem g_plain(const u16* A, int lda, int afar, const u16* W, int ldw, u16* C, int ldc, int nk) {
  GItem it;
  it.A = A; it.W = W; it.C = C; it.lda = lda; it.ldw = ldw; it.ldc = ldc; it.nk = nk; it.afar = afar;
  it.c16 = 16 * ldc; it.c32 = 32 * ldc; it.c128 = 128 * ldc; it.cwin = -1; it.cjump = 128; it.cpanel = 0;
  it.scale = 1.f; it.flag = -1; it.two = 0;
  return it;
}
struct AJump {
  int ja, jf, tog;
  __device__ __forceinline__ void set(int afar) { ja = afar < 0 ? 128 : (1 << 20) - 128; jf = afar < 0 ? 0 : afar ? -8192 : 8192; tog = 0; }
  __device__ __forceinline__ int next() {
    const int j = ja + (tog ? jf : 0);
    jf = tog ? -jf : jf;
    tog ^= 1;
    return j;
  }
};
__device__ __forceinline__ int a_bytes(const GItem& it, int groups) {
  return it.afar < 0 ? ((groups * 32 - 1) * it.lda + it.nk * 64) * 2 : ((it.nk / 2 - 1) << 20) + groups * 16384;
}
__device__ __forceinline__ int a_group(const GItem& it) { return it.afar < 0 ? 64 * it.lda : 16384; }
struct GStream {
  __amdgpu_buffer_rsrc_t ra0, ra1, rw;
  int va0[4], va1[4], vw[4];
  int sa0, sa1, sw;
  int ws;
  AJump ja0, ja1;
};
struct GCtx {
  LAS unsigned char* L;
  unsigned ldsb;
  int lane, w;
  int aoff[2], boff[4];
};
__device__ __forceinline__ void gs_set_a0(GStream& g, const GItem& it, bool valid, int lane, int w) {
  g.ra0 = mk_rsrc(it.A, valid ? a_bytes(it, 8) : 0);
#pragma unroll
  for (int e = 0; e < 4; ++e) g.va0[e] = w * a_group(it) + (8 * e + (lane >> 3)) * it.lda * 2 + (((lane & 7) ^ (4 * (e & 1) + (lane >> 4))) * 16);
  g.sa0 = max(it.afar, 0);
  g.ja0.set(it.afar);
  asm volatile("" : "+s"(g.sa0), "+s"(g.ja0.ja), "+s"(g.ja0.jf));
}
__device__ __forceinline__ void gs_set_rest(GStream& g, const GItem& it, bool valid, int lane, int w) {
  g.ra1 = mk_rsrc(it.A, valid ? a_bytes(it, 8) : 0);
#pragma unroll
  for (int e = 0; e < 4; ++e) g.va1[e] = (4 + w) * a_group(it) + (8 * e + (lane >> 3)) * it.lda * 2 + (((lane & 7) ^ (4 * (e & 1) + (lane >> 4))) * 16);
  g.sa1 = max(it.afar, 0);
  g.ja1.set(it.afar);
  g.rw = mk_rsrc(it.W, valid ? ((it.nk * 64 - 1) * it.ldw + 256) * 2 : 0);
#pragma unroll
  for (int e = 0; e < 4; ++e) g.vw[e] = (16 * w + 4 * e + (lane >> 4)) * it.ldw * 2 + (((lane & 15) ^ ((lane >> 4) << 2) ^ (e & 2)) * 16);
  g.sw = 0;
  g.ws = 128 * it.ldw;
  asm volatile("" : "+s"(g.sa1), "+s"(g.sw), "+s"(g.ws), "+s"(g.ja1.ja), "+s"(g.ja1.jf));
}
template <int KIND, int P>
__device__ __forceinline__ void g_m0(const GCtx& c, int i) {
  constexpr unsigned slot = KIND < 2 ? (P * 2 + KIND) * 16384u : 65536u + (P * 2 + KIND - 2) * 16384u;
  if (i == 0) dma_m0<slot>(c.ldsb);
  else if (i == 1) dma_m0<slot + 1024>(c.ldsb);
  else if (i == 2) dma_m0<slot + 2048>(c.ldsb);
  else dma_m0<slot + 3072>(c.ldsb);
}
template <int KIND>
__device__ __forceinline__ void g_go(const GStream& g, int sw1, int i) {
  if (KIND == 0) dma_go1(g.va0[i], g.ra0, g.sa0);
  else if (KIND == 1) dma_go1(g.va1[i], g.ra1, g.sa1);
  else dma_go(g.vw[i], g.rw, KIND == 2 ? g.sw : sw1);
}
template <int KIND, int P>
__device__ __forceinline__ void g_dma(const GCtx& c, const GStream& g, int i) {
  g_m0<KIND, P>(c, i);
  NOPS(5);
  g_go<KIND>(g, g.sw + 256, i);
}
template <int H, int P>
__device__ __forceinline__ v4u g_rda(const GCtx& c, int mt, int ks) {
  return *(LAS v4u*)(c.L + c.aoff[ks] + (P * 2 + H) * 16384 + mt * 2048);
}
template <int H, int P>
__device__ __forceinline__ v4u g_rdb(const GCtx& c, int nt, int ks) {
  LAS unsigned char* p = c.L + c.boff[nt] + (P * 2 + H) * 16384 + ks * 8192;
  const v2u lo = trread(p), hi = trread(p + 1024);
  return v4u{lo.x, lo.y, hi.x, hi.y};
}
typedef f32x4 GAcc[4][4];
typedef v4u GFrag[4][2];
template <bool ZERO, int RK, int RP, int DK, int DP>
__device__ __forceinline__ void g_phase(const GCtx& c, const GStream& g, GAcc& acc, const GFrag& af, const GFrag& bf, GFrag& rf) {
  const int sw1 = g.sw + 256;
#pragma unroll
  for (int n = 0; n < 32; ++n) {
    const int ks = n >> 4, nt = (n >> 2) & 3, mt = n & 3;
    if (ZERO && ks == 0) asm volatile("v_mfma_f32_16x16x32_bf16 %0, %1, %2, 0" : "=a"(acc[nt][mt]) : "v"(bf[nt][ks]), "v"(af[mt][ks]));
    else asm volatile("v_mfma_f32_16x16x32_bf16 %0, %1, %2, %0" : "+a"(acc[nt][mt]) : "v"(bf[nt][ks]), "v"(af[mt][ks]));
    if (n < 16 && (n & 1) == 0) {
      if (RK < 2) rf[(n >> 1) & 3][n >> 3] = g_rda<RK & 1, RP>(c, (n >> 1) & 3, n >> 3);
      else rf[(n >> 1) & 3][n >> 3] = g_rdb<RK & 1, RP>(c, (n >> 1) & 3, n >> 3);
    }
    if (n >= GDMA0 && n < GDMA0 + 4 * GDMAS && ((n - GDMA0) % GDMAS) == 0) g_m0<DK, DP>(c, (n - GDMA0) / GDMAS);
    if (n > GDMA0 && n <= GDMA0 + 4 * GDMAS && ((n - GDMA0) % GDMAS) == GDMAL) g_go<DK>(g, sw1, (n - GDMA0) / GDMAS);
    if (n == GBAR) asm volatile("s_waitcnt vmcnt(24) lgkmcnt(0)\n\ts_barrier" ::: "memory");
    SB();
  }
}
template <bool FIRST>
__device__ __forceinline__ void g_pair(const GCtx& c, GStream& g, GAcc (&acc)[2][2], GFrag& a0, GFrag& a1, GFrag& bx, GFrag& by, bool sw, const GItem& nx,
                                       bool nvalid) {
  g_phase<FIRST, 3, 0, 2, 0>(c, g, acc[0][0], a0, bx, by);
  g_phase<FIRST, 1, 0, 3, 0>(c, g, acc[0][1], a0, by, a1);
  g.sw += g.ws;
  g_phase<FIRST, 0, 1, 1, 0>(c, g, acc[1][1], a1, by, a0);
  g.sa1 += 128;
  g_phase<FIRST, 2, 1, 0, 1>(c, g, acc[1][0], a1, bx, by);
  g.sa0 += g.ja0.next();
  if (sw) gs_set_a0(g, nx, nvalid, c.lane, c.w);
  g_phase<false, 3, 1, 2, 1>(c, g, acc[0][0], a0, by, bx);
  g_phase<false, 1, 1, 3, 1>(c, g, acc[0][1], a0, bx, a1);
  g.sw += g.ws;
  g_phase<false, 0, 0, 1, 1>(c, g, acc[1][1], a1, bx, a0);
  g.sa1 += g.ja1.next();
  g_phase<false, 2, 0, 0, 0>(c, g, acc[1][0], a1, by, bx);
  g.sa0 += 128;
  if (sw) gs_set_rest(g, nx, nvalid, c.lane, c.w);
}
template <int AUX, int NQ>
__device__ __forceinline__ void g_epilogue(const GCtx& c, const GItem& it, GAcc (&acc)[NQ][NQ], gu32* ctl, unsigned& pol) {
  if (pol > 1u) {
    bool dead = false;
    for (unsigned spins = 0; (pol = __builtin_amdgcn_readfirstlane(__hip_atomic_load(ctl + CTL_POL, RLX_AGENT))) > 1u;)
      if (spin_fail(spins, ctl, dead)) break;
    pol &= 1u;
  }
  int lane = c.lane;
  NOPS(8);
  PIN1(lane);
  const int m = lane & 15, gq = lane >> 4, wr = c.w >> 1, wc = c.w & 1;
  LAS unsigned char* st = c.L + G_STAGE + c.w * 4096;
  const bool pan = it.cpanel != 0;
  __amdgpu_buffer_rsrc_t rc = mk_rsrc((unsigned char*)it.C + (it.cwin >= 0 ? far_off(pol, it.cwin) : 0),
      ((NQ - 1) * (it.c128 + it.cjump) + 3 * it.c32 + it.c16 + 15 * it.ldc + (pan ? 3 * it.cpanel + 32 : 128)) * 2);
  const int rrow = pan ? lane >> 2 : lane >> 3, rch = pan ? lane & 3 : lane & 7;
  const int vc = (wr * 2 * it.c32 + rrow * it.ldc + (pan ? 2 * wc * it.cpanel : wc * 64) + rch * 8) * 2;
  const int rd0 = rrow * 128 + ((rch ^ (rrow & 7)) * 16), rd1 = pan ? rd0 ^ 64 : rd0 + 1024;
  const int q1 = pan ? it.cpanel : 8 * it.ldc;
  const float sc = it.scale;
#pragma unroll
  for (int i = 0; i < NQ; ++i)
#pragma unroll
    for (int j = 0; j < NQ; ++j)
#pragma unroll
      for (int mh = 0; mh < 2; ++mh) {
#pragma unroll
        for (int mt = 2 * mh; mt < 2 * mh + 2; ++mt) {
          asm volatile("" : "+a"(acc[i][j][0][mt]), "+a"(acc[i][j][1][mt]), "+a"(acc[i][j][2][mt]), "+a"(acc[i][j][3][mt]));
#pragma unroll
          for (int nt = 0; nt < 4; ++nt) {
            const f32x4& v = acc[i][j][nt][mt];
            *(LAS v2u*)(st + ((mt & 1) * 16 + m) * 128 + (((nt * 2 + (gq >> 1)) ^ (m & 7)) * 16) + (gq & 1) * 8) =
                v2u{cvt_pk(v[0] * sc, v[1] * sc), cvt_pk(v[2] * sc, v[3] * sc)};
          }
        }
#pragma unroll
        for (int q = 0; q < 4; ++q) {
          const v4u o = *(LAS v4u*)(st + ((q & 1) ? rd1 : rd0) + (q >> 1) * 2048);
          __builtin_amdgcn_raw_buffer_store_b128(o, rc, vc, (i * it.c128 + mh * it.c32 + (q >> 1) * it.c16 + (q & 1) * q1 + j * it.cjump) * 2, AUX);
        }
        SB();
      }
}
__device__ __forceinline__ void g_publish(gu32* ctl, int flag, int two) {
  if (flag >= 0 && threadIdx.x == 0) {
    __hip_atomic_fetch_add(ctl + flag, two ? 2u : 1u, RLX_AGENT);
    if (two) __hip_atomic_fetch_add(ctl + flag + 64, 2u, RLX_AGENT);
  }
}
__device__ __forceinline__ void g_ctx(GCtx& c, LAS unsigned char* L) {
  c.L = L;
  c.lane = threadIdx.x & 63;
  PIN1(c.lane);
  c.w = __builtin_amdgcn_readfirstlane(threadIdx.x >> 6);
  c.ldsb = (unsigned)(size_t)L + c.w * 4096u;
  const int lane = c.lane, wr = c.w >> 1, wc = c.w & 1;
#pragma unroll
  for (int ks = 0; ks < 2; ++ks) c.aoff[ks] = (wr * 64 + (lane & 15)) * 128 + (((4 * ks + (lane >> 4)) ^ ((lane >> 1) & 7)) * 16);
#pragma unroll
  for (int nt = 0; nt < 4; ++nt)
    c.boff[nt] = 65536 + (8 * (lane >> 4) + ((lane >> 2) & 3)) * 256 +
                 (((8 * wc + 2 * nt + ((lane >> 1) & 1)) ^ (((lane >> 2) & 3) << 2) ^ (((lane >> 4) & 1) << 1)) * 16) + (lane & 1) * 8;
}
template <int AUX, class F>
__device__ __forceinline__ void gemm_tiles(LAS unsigned char* L, int n, F item, gu32* ctl, unsigned& pol) {
  if (n <= 0) return;
  GCtx c;
  g_ctx(c, L);
  const int lane = c.lane;
  GItem cur = item(0);
  GStream g;
  gs_set_a0(g, cur, true, lane, c.w);
  gs_set_rest(g, cur, true, lane, c.w);
  GFrag a0, a1, bx, by;
  GAcc acc[2][2];
  asm volatile("s_waitcnt vmcnt(0) lgkmcnt(0)\n\ts_barrier" ::: "memory");
#pragma unroll
  for (int i = 0; i < 4; ++i) g_dma<0, 0>(c, g, i);
#pragma unroll
  for (int i = 0; i < 4; ++i) g_dma<2, 0>(c, g, i);
#pragma unroll
  for (int i = 0; i < 4; ++i) g_dma<3, 0>(c, g, i);
#pragma unroll
  for (int i = 0; i < 4; ++i) g_dma<1, 0>(c, g, i);
  g.sa0 += 128; g.sa1 += 128; g.sw += g.ws;
#pragma unroll
  for (int i = 0; i < 4; ++i) g_dma<0, 1>(c, g, i);
#pragma unroll
  for (int i = 0; i < 4; ++i) g_dma<2, 1>(c, g, i);
#pragma unroll
  for (int i = 0; i < 4; ++i) g_dma<3, 1>(c, g, i);
#pragma unroll
  for (int i = 0; i < 4; ++i) g_dma<1, 1>(c, g, i);
  g.sa0 += g.ja0.next(); g.sa1 += g.ja1.next(); g.sw += g.ws;
  asm volatile("s_waitcnt vmcnt(24)\n\ts_barrier" ::: "memory");
#pragma unroll
  for (int k = 0; k < 8; ++k) { a0[k & 3][k >> 2] = g_rda<0, 0>(c, k & 3, k >> 2); bx[k & 3][k >> 2] = g_rdb<0, 0>(c, k & 3, k >> 2); }
  asm volatile("s_waitcnt lgkmcnt(0)\n\ts_barrier" ::: "memory");
#pragma unroll
  for (int i = 0; i < 4; ++i) g_dma<0, 0>(c, g, i);
  g.sa0 += 128;
  asm volatile("s_waitcnt vmcnt(24)\n\ts_barrier" ::: "memory");
  int pend = -1, pend2 = 0;
  for (int k = 0; k < n; ++k) {
    const bool nvalid = k + 1 < n;
    const GItem nx = item(nvalid ? k + 1 : k);
    g_pair<true>(c, g, acc, a0, a1, bx, by, cur.nk == 4, nx, nvalid);
    g_publish(ctl, pend, pend2);
    for (int t = 2; t < cur.nk; t += 2) g_pair<false>(c, g, acc, a0, a1, bx, by, t == cur.nk - 4, nx, nvalid);
    GSTAMP(0);
    g_epilogue<AUX, 2>(c, cur, acc, ctl, pol);
    GSTAMP(1);
    pend = cur.flag;
    pend2 = cur.two;
    cur = nx;
  }
  asm volatile("s_waitcnt vmcnt(0) lgkmcnt(0)\n\ts_barrier" ::: "memory");
  g_publish(ctl, pend, pend2);
}

struct SStream { __amdgpu_buffer_rsrc_t ra, rw; int va[4], vw[4], sa, sw, ws, da; AJump ja; };
__device__ __forceinline__ void ss_set(SStream& g, const GItem& it, bool valid, int lane, int w) {
  g.ra = mk_rsrc(it.A, valid ? a_bytes(it, 4) : 0);
  g.rw = mk_rsrc(it.W, valid ? ((it.nk * 64 - 1) * it.ldw + 128) * 2 : 0);
#pragma unroll
  for (int e = 0; e < 4; ++e) {
    g.va[e] = w * a_group(it) + (8 * e + (lane >> 3)) * it.lda * 2 + (((lane & 7) ^ (4 * (e & 1) + (lane >> 4))) * 16);
    g.vw[e] = (16 * w + 4 * e + (lane >> 4)) * it.ldw * 2 + (((lane & 15) ^ ((lane >> 4) << 2) ^ (e & 2)) * 16);
  }
  g.sa = max(it.afar, 0); g.sw = 0; g.ws = 128 * it.ldw; g.ja.set(it.afar);
  g.da = 128;
  if (it.two) {
    g.sa = (it.nk - 1) * 128; g.sw = (it.nk - 1) * g.ws; g.ws = -g.ws; g.da = -128; g.ja.ja = -128;
  }
  asm volatile("" : "+s"(g.sa), "+s"(g.sw), "+s"(g.ws), "+s"(g.ja.ja), "+s"(g.ja.jf));
}
template <int R>
__device__ __forceinline__ void s_m0(const GCtx& c, int i) {
  if (i < 4) g_m0<R & 1, (R >> 1)>(c, i);
  else g_m0<2 + (R & 1), (R >> 1)>(c, i - 4);
}
__device__ __forceinline__ void s_go(const SStream& g, int i) {
  if (i < 4) dma_go1(g.va[i], g.ra, g.sa);
  else dma_go(g.vw[i - 4], g.rw, g.sw);
}
template <int R>
__device__ __forceinline__ void s_dma(const GCtx& c, const SStream& g, int i) {
  s_m0<R>(c, i);
  NOPS(5);
  s_go(g, i);
}
template <bool ZERO, int R, bool DMA = true>
__device__ __forceinline__ void s_step(const GCtx& c, SStream& g, GAcc& acc, const GFrag& af, const GFrag& bf, GFrag& an, GFrag& bn) {
  constexpr int N = (R + 1) & 3;
#pragma unroll
  for (int n = 0; n < 32; ++n) {
    const int ks = n >> 4, nt = (n >> 2) & 3, mt = n & 3;
    if (ZERO && ks == 0) asm volatile("v_mfma_f32_16x16x32_bf16 %0, %1, %2, 0" : "=a"(acc[nt][mt]) : "v"(bf[nt][ks]), "v"(af[mt][ks]));
    else asm volatile("v_mfma_f32_16x16x32_bf16 %0, %1, %2, %0" : "+a"(acc[nt][mt]) : "v"(bf[nt][ks]), "v"(af[mt][ks]));
    if (n < 24 && n % 3 == 0) an[(n / 3) & 3][(n / 3) >> 2] = g_rda<N & 1, (N >> 1)>(c, (n / 3) & 3, (n / 3) >> 2);
    if (n < 24 && n % 3 == 1) bn[(n / 3) & 3][(n / 3) >> 2] = g_rdb<N & 1, (N >> 1)>(c, (n / 3) & 3, (n / 3) >> 2);
    if (DMA && n < 24 && n % 3 == 0) s_m0<R>(c, n / 3);
    if (DMA && n < 24 && n % 3 == 2) s_go(g, n / 3);
    if (DMA && n == 29) asm volatile("s_waitcnt vmcnt(16) lgkmcnt(0)\n\ts_barrier" ::: "memory");
    SB();
  }
  g.sa += (R & 1) ? g.ja.next() : g.da;
  g.sw += g.ws;
}
template <int AUX, class F>
__device__ __forceinline__ void gemm_small(LAS unsigned char* L, int n, F item, gu32* ctl, unsigned& pol) {
  if (n <= 0) return;
  GCtx c;
  g_ctx(c, L);
  GItem cur = item(0);
  SStream g;
  ss_set(g, cur, true, c.lane, c.w);
  GFrag a0, a1, b0, b1;
  GAcc acc[1][1];
  asm volatile("s_waitcnt vmcnt(0) lgkmcnt(0)\n\ts_barrier" ::: "memory");
#pragma unroll
  for (int i = 0; i < 8; ++i) s_dma<0>(c, g, i);
  g.sa += 128; g.sw += g.ws;
#pragma unroll
  for (int i = 0; i < 8; ++i) s_dma<1>(c, g, i);
  g.sa += g.ja.next(); g.sw += g.ws;
#pragma unroll
  for (int i = 0; i < 8; ++i) s_dma<2>(c, g, i);
  g.sa += 128; g.sw += g.ws;
#pragma unroll
  for (int i = 0; i < 8; ++i) s_dma<3>(c, g, i);
  g.sa += g.ja.next(); g.sw += g.ws;
  asm volatile("s_waitcnt vmcnt(24)\n\ts_barrier" ::: "memory");
#pragma unroll
  for (int k = 0; k < 8; ++k) { a0[k & 3][k >> 2] = g_rda<0, 0>(c, k & 3, k >> 2); b0[k & 3][k >> 2] = g_rdb<0, 0>(c, k & 3, k >> 2); }
  asm volatile("s_waitcnt vmcnt(16) lgkmcnt(0)\n\ts_barrier" ::: "memory");
  int pend = -1;
  for (int k = 0; k < n; ++k) {
    const bool nvalid = k + 1 < n;
    const GItem nx = item(nvalid ? k + 1 : k);
    s_step<true, 0>(c, g, acc[0][0], a0, b0, a1, b1);
    s_step<false, 1>(c, g, acc[0][0], a1, b1, a0, b0);
    s_step<false, 2>(c, g, acc[0][0], a0, b0, a1, b1);
    g_publish(ctl, pend, 0);
    s_step<false, 3>(c, g, acc[0][0], a1, b1, a0, b0);
    for (int t = 4; t < cur.nk; t += 4) {
      if (t == cur.nk - 4) ss_set(g, nx, nvalid, c.lane, c.w);
      s_step<false, 0>(c, g, acc[0][0], a0, b0, a1, b1);
      s_step<false, 1>(c, g, acc[0][0], a1, b1, a0, b0);
      s_step<false, 2>(c, g, acc[0][0], a0, b0, a1, b1);
      s_step<false, 3>(c, g, acc[0][0], a1, b1, a0, b0);
    }
    g_epilogue<AUX, 1>(c, cur, acc, ctl, pol);
    SSTAMP();
    pend = cur.flag;
    if (k < 2 && nvalid) {
      asm volatile("s_waitcnt vmcnt(0)\n\ts_barrier" ::: "memory");
      g_publish(ctl, pend, 0);
      pend = -1;
    }
    cur = nx;
  }
  asm volatile("s_waitcnt vmcnt(0) lgkmcnt(0)\n\ts_barrier" ::: "memory");
  g_publish(ctl, pend, 0);
}

template <int AUX, class N>
__device__ __forceinline__ void gemm_last(LAS unsigned char* L, const GItem& it0, int cstep, N next, gu32* ctl, unsigned& pol) {
  GItem it = it0;
  GCtx c;
  g_ctx(c, L);
  SStream g;
  ss_set(g, it, true, c.lane, c.w);
  asm volatile("s_waitcnt vmcnt(0) lgkmcnt(0)\n\ts_barrier" ::: "memory");
#pragma unroll
  for (int i = 4; i < 8; ++i) s_dma<0>(c, g, i);
  g.sw += g.ws;
#pragma unroll
  for (int i = 4; i < 8; ++i) s_dma<1>(c, g, i);
  g.sw += g.ws;
#pragma unroll
  for (int i = 4; i < 8; ++i) s_dma<2>(c, g, i);
  g.sw += g.ws;
#pragma unroll
  for (int i = 4; i < 8; ++i) s_dma<3>(c, g, i);
  for (;;) {
    GFrag a0, a1, b0, b1;
    GAcc acc[1][1];
    LSTAMP(0);
    const int j = next();
    if (j < 0) break;
    LSTAMP(1);
    it.A = it0.A + j * 4 * 8192;
    it.C = it0.C + j * cstep;
    it.flag = it0.flag + j * 8 * 64;
    ss_set(g, it, true, c.lane, c.w);
#pragma unroll
    for (int i = 0; i < 4; ++i) s_dma<0>(c, g, i);
    g.sa += 128;
#pragma unroll
    for (int i = 0; i < 4; ++i) s_dma<1>(c, g, i);
    g.sa += g.ja.next();
#pragma unroll
    for (int i = 0; i < 4; ++i) s_dma<2>(c, g, i);
    g.sa += 128;
#pragma unroll
    for (int i = 0; i < 4; ++i) s_dma<3>(c, g, i);
    asm volatile("s_waitcnt vmcnt(0)\n\ts_barrier" ::: "memory");
#pragma unroll
    for (int k = 0; k < 8; ++k) { a0[k & 3][k >> 2] = g_rda<0, 0>(c, k & 3, k >> 2); b0[k & 3][k >> 2] = g_rdb<0, 0>(c, k & 3, k >> 2); }
    LSTAMP(2);
    s_step<true, 0, false>(c, g, acc[0][0], a0, b0, a1, b1);
    s_step<false, 1, false>(c, g, acc[0][0], a1, b1, a0, b0);
    s_step<false, 2, false>(c, g, acc[0][0], a0, b0, a1, b1);
    s_step<false, 3, false>(c, g, acc[0][0], a1, b1, a0, b0);
    LSTAMP(3);
    g_epilogue<AUX, 1>(c, it, acc, ctl, pol);
    LSTAMP(4);
    asm volatile("s_waitcnt vmcnt(0) lgkmcnt(0)\n\ts_barrier" ::: "memory");
    g_publish(ctl, it.flag, 0);
    LSTAMP(5);
  }
}

#define ASTAMP(k)
#define MF32_C(d, a, b, c) asm volatile("v_mfma_f32_32x32x16_bf16 %0, %1, %2, %3" : "=&v"(d) : "v"(a), "v"(b), "v"(c))
#define MF32_V(d, a, b) asm volatile("v_mfma_f32_32x32x16_bf16 %0, %1, %2, %0" : "+v"(d) : "v"(a), "v"(b))
#define MF32_A(d, a, b) asm volatile("v_mfma_f32_32x32x16_bf16 %0, %1, %2, %0" : "+a"(d) : "v"(a), "v"(b))
struct ACtx {
  LAS unsigned char* L;
  unsigned ldsb;
  __amdgpu_buffer_rsrc_t rkv;
  int vk, vv;
  int koff[2], voff;
  v4u q[2][2], ones;
  v4u kf[2][2], vf[2][2], pb[2][2];
  f32x16 s[2], negr[2], o[2], l[2];
  float t[4];
};
__device__ __forceinline__ void a_dma(ACtx& c, int stage, int i) {
  dma16(c.ldsb + (stage & 3) * 16384 + (i >> 1) * 8192 + (i & 1) * 1024, i < 2 ? c.vk : c.vv, c.rkv, stage * 16384 + (i & 1) * 1024);
}
__device__ __forceinline__ v4u a_rdk(ACtx& c, int ko, int blk) { return *(LAS v4u*)(c.L + ko + blk * 2048); }
__device__ __forceinline__ v4u a_rdv(ACtx& c, int vo_, int blk, int s) {
  LAS unsigned char* p = c.L + vo_ + blk * 2048 + s * 1024;
  const v2u lo = trread(p), hi = trread(p + 512);
  return v4u{lo.x, lo.y, hi.x, hi.y};
}
template <int QT>
__device__ __forceinline__ void a_ref(ACtx& c) {
  float mx = c.s[QT][0];
#pragma unroll
  for (int r = 1; r < 16; ++r) mx = fmx(mx, c.s[QT][r]);
  auto sw = __builtin_amdgcn_permlane32_swap(ubits(mx), ubits(mx), false, false);
  const float d = fmx(fbits(sw[0]), fbits(sw[1]));
  c.negr[QT] -= d;
  c.s[QT] -= d;
  asm volatile("s_nop 1" : "+v"(c.negr[QT]), "+v"(c.s[QT]));
}
template <int QT, int BLK, int MODE>
__device__ __forceinline__ void a_tile(ACtx& c, int k0, int k1, int v0, int dstage) {
  constexpr int P = QT ^ 1, KN = (BLK + QT) & 1, V1 = (BLK + QT + 1) & 1, V2 = (BLK + 1) & 1;
  constexpr bool H1 = MODE & 1, H2 = MODE & 2, H0 = MODE & 4;
  float e[16];
  PIN1(c.s[QT]);
  auto mf = [&](int j) {
    if (j == 0 && H2) MF32_A(c.o[QT], c.vf[V2][1], c.pb[QT][1]);
    if (j == 4 && H2) MF32_A(c.l[QT], c.ones, c.pb[QT][1]);
    if (j == 8 && H0) MF32_C(c.s[P], c.kf[KN][0], c.q[P][0], c.negr[P]);
    if (j == 12 && H0) MF32_V(c.s[P], c.kf[KN][1], c.q[P][1]);
    if (j == 16 && H1) MF32_A(c.o[P], c.vf[V1][0], c.pb[P][0]);
    if (j == 20 && H1) MF32_A(c.l[P], c.ones, c.pb[P][0]);
    SB();
  };
#pragma unroll
  for (int i = 0; i < 8; ++i) {
    mf(3 * i);
    if (H0) { e[2 * i] = __builtin_amdgcn_exp2f(c.s[QT][2 * i]); if (i >= 6) PIN1(e[2 * i]); SB(); }
    mf(3 * i + 1);
    if (i >= 2) { if (H0) c.pb[QT][(i - 2) >> 2][(i - 2) & 3] = cvt_pk(e[2 * i - 4], e[2 * i - 3]); }
    else if (H1) c.pb[P][1][2 + i] = cvt_pk(c.t[2 * i], c.t[2 * i + 1]);
    SB();
    mf(3 * i + 2);
    if (H0) { e[2 * i + 1] = __builtin_amdgcn_exp2f(c.s[QT][2 * i + 1]); if (i >= 6) PIN1(e[2 * i + 1]); SB(); }
    if (QT == 0 && H0) {
      if (i == 2) c.kf[(BLK + 1) & 1][0] = a_rdk(c, k0, (BLK + 1) & 3);
      if (i == 4) c.kf[(BLK + 1) & 1][1] = a_rdk(c, k1, (BLK + 1) & 3);
      if (i == 6) c.vf[BLK & 1][0] = a_rdv(c, v0, BLK, 0);
    }
    if (QT == 1 && H0) {
      if (i == 2) c.vf[BLK & 1][1] = a_rdv(c, v0, BLK, 1);
      if (i == 6) a_dma(c, dstage, BLK);
    }
    SB();
  }
  if (H0) {
#pragma unroll
    for (int j = 0; j < 4; ++j) c.t[j] = e[12 + j];
  }
}
template <int AUX>
__device__ __forceinline__ void a_store(u16* O, const f32x16 (&o)[2], const f32x16 (&l)[2], int el) {
  __amdgpu_buffer_rsrc_t ro = mk_rsrc(O, 2048 * 1024 * 2);
#pragma unroll
  for (int qt = 0; qt < 2; ++qt) {
    const float ri = __builtin_amdgcn_rcpf(l[qt][0]);
    unsigned d[8];
#pragma unroll
    for (int i = 0; i < 8; ++i) d[i] = cvt_pk(o[qt][2 * i] * ri, o[qt][2 * i + 1] * ri);
#pragma unroll
    for (int k = 0; k < 2; ++k) {
      auto x = __builtin_amdgcn_permlane32_swap(d[4 * k], d[4 * k + 2], false, false);
      auto y = __builtin_amdgcn_permlane32_swap(d[4 * k + 1], d[4 * k + 3], false, false);
      __builtin_amdgcn_raw_buffer_store_b128(v4u{x[0], y[0], x[1], y[1]}, ro, (el + qt * 32 * 1024 + k * 16) * 2, 0, AUX);
    }
  }
}
__device__ __forceinline__ void attn_safe(ACtx& c, f32x16 (&o)[2], f32x16 (&l)[2]) {
  float m[2] = {-3.0e38f, -3.0e38f};
#pragma unroll
  for (int qt = 0; qt < 2; ++qt)
#pragma unroll
    for (int r = 0; r < 16; ++r) { o[qt][r] = 0.f; l[qt][r] = 0.f; }
  for (int b = 0; b < 64; ++b) {
    if ((b & 3) == 0) {
      asm volatile("s_waitcnt lgkmcnt(0)\n\ts_barrier" ::: "memory");
      for (int i = 0; i < 4; ++i) dma16(c.ldsb + (i >> 1) * 8192 + (i & 1) * 1024, i < 2 ? c.vk : c.vv, c.rkv, (b >> 2) * 16384 + (i & 1) * 1024);
      asm volatile("s_waitcnt vmcnt(0)\n\ts_barrier" ::: "memory");
    }
    const v4u ka = a_rdk(c, c.koff[0], b & 3), kb = a_rdk(c, c.koff[1], b & 3), va = a_rdv(c, c.voff, b & 3, 0), vb = a_rdv(c, c.voff, b & 3, 1);
#pragma unroll
    for (int qt = 0; qt < 2; ++qt) {
      f32x16 s;
#pragma unroll
      for (int r = 0; r < 16; ++r) s[r] = 0.f;
      s = mfma32(kb, c.q[qt][1], mfma32(ka, c.q[qt][0], s));
      float mx = m[qt];
#pragma unroll
      for (int r = 0; r < 16; ++r) mx = fmx(mx, s[r]);
      auto sw = __builtin_amdgcn_permlane32_swap(ubits(mx), ubits(mx), false, false);
      mx = fmx(fbits(sw[0]), fbits(sw[1]));
      const float f = __builtin_amdgcn_exp2f(m[qt] - mx);
      m[qt] = mx;
      v4u p[2];
#pragma unroll
      for (int i = 0; i < 8; ++i) p[i >> 2][i & 3] = cvt_pk(__builtin_amdgcn_exp2f(s[2 * i] - mx), __builtin_amdgcn_exp2f(s[2 * i + 1] - mx));
      o[qt] = mfma32(vb, p[1], mfma32(va, p[0], o[qt] * f));
      l[qt] = mfma32(c.ones, p[1], mfma32(c.ones, p[0], l[qt] * f));
    }
  }
}
template <int AUX>
__device__ __forceinline__ void attn_item(LAS unsigned char* L, const unsigned char* QKV, u16* O, int hd, int qb) {
  ACtx c;
  ASTAMP(0);
  int lane = threadIdx.x & 63;
  PIN1(lane);
  const int w = __builtin_amdgcn_readfirstlane(threadIdx.x >> 6), hh = lane >> 5;
  c.L = L;
  c.ldsb = (unsigned)(size_t)L + w * 2048u;
  c.rkv = mk_rsrc(QKV + ((size_t)(32 + hd) << 18), (33 << 18) - 8192);
  c.vk = (32 * w + (lane >> 2)) * 64 + (((lane & 3) ^ (lane >> 4)) * 16);
  c.vv = (32 << 18) + 32 * w * 64 + lane * 16;
#pragma unroll
  for (int ks = 0; ks < 2; ++ks) c.koff[ks] = (lane & 31) * 64 + (((2 * ks + hh) ^ ((lane >> 2) & 3)) * 16);
  c.voff = 8192 + (4 * hh + ((lane >> 2) & 3)) * 64 + ((lane >> 4) & 1) * 32 + (lane & 3) * 8;
  asm volatile("s_waitcnt vmcnt(0) lgkmcnt(0)\n\ts_barrier" ::: "memory");
  const int q0 = qb * 256 + w * 64;
  __amdgpu_buffer_rsrc_t rq = mk_rsrc(QKV + ((size_t)hd << 18), (1 << 18) - 8192);
#pragma unroll
  for (int qt = 0; qt < 2; ++qt)
#pragma unroll
    for (int ks = 0; ks < 2; ++ks)
      c.q[qt][ks] = __builtin_amdgcn_raw_buffer_load_b128(rq, ((q0 & 64) + qt * 32 + (lane & 31)) * 64 + ks * 32 + hh * 16, (q0 >> 7) * 16384, AUX_SC1);
  SB();
#pragma unroll
  for (int st = 0; st < 2; ++st)
#pragma unroll
    for (int i = 0; i < 4; ++i) a_dma(c, st, i);
  c.ones = v4u{0x3F803F80u, 0x3F803F80u, 0x3F803F80u, 0x3F803F80u};
#pragma unroll
  for (int qt = 0; qt < 2; ++qt) {
#pragma unroll
    for (int r = 0; r < 16; ++r) { c.negr[qt][r] = 0.f; c.o[qt][r] = 0.f; c.l[qt][r] = 0.f; }
    asm volatile("" : "+v"(c.negr[qt]), "+a"(c.o[qt]), "+a"(c.l[qt]));
  }
  PIN1(c.ones);
  asm volatile("s_waitcnt vmcnt(4)\n\ts_barrier" : "+v"(c.q[0][0]), "+v"(c.q[0][1]), "+v"(c.q[1][0]), "+v"(c.q[1][1])::"memory");
  c.kf[0][0] = a_rdk(c, c.koff[0], 0);
  c.kf[0][1] = a_rdk(c, c.koff[1], 0);
  MF32_C(c.s[1], c.kf[0][0], c.q[1][0], c.negr[1]);
  MF32_V(c.s[1], c.kf[0][1], c.q[1][1]);
  MF32_C(c.s[0], c.kf[0][0], c.q[0][0], c.negr[0]);
  MF32_V(c.s[0], c.kf[0][1], c.q[0][1]);
  NOPS(12);
  PIN2(c.s[0], c.s[1]);
  a_ref<1>(c);
  a_ref<0>(c);
  ASTAMP(1);
  a_tile<0, 0, 4>(c, c.koff[0], c.koff[1], c.voff, 2);
  a_tile<1, 0, 5>(c, c.koff[0], c.koff[1], c.voff, 2);
  auto six = [&](int j) {
    const int so = (j & 3) * 16384, sn = ((j + 1) & 3) * 16384, ds = min(j + 2, 15);
    const int k0 = c.koff[0] + so, k1 = c.koff[1] + so, v0 = c.voff + so;
    a_tile<0, 1, 7>(c, k0, k1, v0, ds);
    a_tile<1, 1, 7>(c, k0, k1, v0, ds);
    a_tile<0, 2, 7>(c, k0, k1, v0, ds);
    asm volatile("s_waitcnt vmcnt(2)\n\ts_barrier" ::: "memory");
    a_tile<1, 2, 7>(c, k0, k1, v0, ds);
    a_tile<0, 3, 7>(c, c.koff[0] + sn, c.koff[1] + sn, v0, ds);
    a_tile<1, 3, 7>(c, k0, k1, v0, ds);
  };
  for (int j = 0; j < 15; ++j) {
    six(j);
    const int sn = ((j + 1) & 3) * 16384;
    a_tile<0, 0, 7>(c, c.koff[0] + sn, c.koff[1] + sn, c.voff + sn, min(j + 3, 15));
    a_tile<1, 0, 7>(c, c.koff[0] + sn, c.koff[1] + sn, c.voff + sn, min(j + 3, 15));
  }
  six(15);
  a_tile<0, 0, 3>(c, 0, 0, 0, 0);
  NOPS(2);
  MF32_A(c.o[1], c.vf[1][1], c.pb[1][1]);
  MF32_A(c.l[1], c.ones, c.pb[1][1]);
  NOPS(12);
  asm volatile("" : "+a"(c.o[0]), "+a"(c.l[0]), "+a"(c.o[1]), "+a"(c.l[1]), "+v"(c.s[0]), "+v"(c.negr[0]), "+v"(c.negr[1]));
  ASTAMP(2);
  bool bad = false;
#pragma unroll
  for (int qt = 0; qt < 2; ++qt) {
    float m = c.l[qt][0];
#pragma unroll
    for (int r = 0; r < 16; ++r) m = fmx(m, __builtin_fabsf(c.o[qt][r]));
    bad |= !(m < 1e30f);
  }
  LAS unsigned* vote = (LAS unsigned*)(L + G_STAGE);
  if (lane == 0) vote[w] = 0u;
  if (bad) vote[w] = 1u;
  asm volatile("s_waitcnt vmcnt(0) lgkmcnt(0)\n\ts_barrier" ::: "memory");
  const v4u vt = *(LAS v4u*)vote;
  if (FORCE_SAFE || __builtin_amdgcn_readfirstlane(vt.x | vt.y | vt.z | vt.w)) attn_safe(c, c.o, c.l);
  a_store<AUX>(O, c.o, c.l, (q0 + (lane & 31)) * 1024 + hd * 32 + hh * 8);
  ASTAMP(3);
}

#define G4FIRST 48
#define SREV 1
#define PSL 4
struct Args {
  const u16* Ax; const u16* Wq; const u16* Wk; const u16* Wv; const u16* Wo; const u16* Bx; const u16* Wb; const u16* Wob;
  u16* out; unsigned char* base;
};
constexpr float CB = 0.17677669529663687f * 1.4426950408889634f;
__device__ __forceinline__ GItem proj_item(const Args& a, int x, int id) {
  GItem it;
  if (id < 96) {
    const int rb = id / 12, ct = (id % 12) * 4 + x, m = ct >> 4;
    it = g_plain(a.Ax + (size_t)rb * 256 * 1024, 1024, -1, (m == 0 ? a.Wq : m == 1 ? a.Wk : a.Wv) + (ct & 15) * 256, 4096,
                 (u16*)(a.base + XQKV_OFF + ((size_t)ct << 21) + rb * 8 * 16384), 128, 16);
    it.c32 = 8192; it.c128 = 32768; it.cjump = 1 << 19; it.cwin = (int)(XQKV_OFF >> 21) + ct;
    if (m == 0) it.scale = CEXP;
    it.flag = CTL_RB + 128 * rb;
    it.two = 1;
  } else {
    const int rb = (id - 96) / 3, ct = ((id - 96) % 3) * 4 + x;
    it = g_plain(a.Bx + (size_t)rb * 256 * 1024, 1024, -1, a.Wb + ct * 256, 3072, (u16*)(a.base + BQKV_OFF + ((size_t)ct << 21) + rb * 2 * 16384), 32, 16);
    it.c128 = 8192; it.cpanel = 1 << 17; it.cjump = 1 << 19; it.cwin = (int)(BQKV_OFF >> 21) + ct;
    if (ct < 4) it.scale = CB;
    it.flag = CTL_B;
  }
  return it;
}
__device__ __forceinline__ GItem small_item(const Args& a, int x, int i, int k) {
  const int id = k < 2 ? i >> 1 : (k - 1) * 12 + 8 + (i >> 2), rh = k < 2 ? k : (i >> 1) & 1, ch = i & 1;
  GItem it = proj_item(a, x, id);
  it.A += rh * 128 * 1024;
  it.W += ch * 128;
  it.C += rh * it.c128 + ch * it.cjump;
  it.flag += 64 * rh;
  it.two = SREV && k == 1;
  return it;
}
__device__ __forceinline__ int big_id(int i, int k) {
  if (i < 16) return k == 0 ? 44 + i : k == 1 ? (i < 8 ? 112 + i : 52 + i) : 80 + i;
  if (i < 24) return k == 0 ? 8 + i : k == 1 ? 80 + i : 52 + i;
  return k == 0 ? i - 12 : k == 1 ? 12 + i : k == 2 ? 80 + i : 52 + i;
}
__device__ __forceinline__ void wg_wait(gu32* ctl, int word, unsigned want) {
  if (threadIdx.x < 64) {
    bool dead = false;
    for (unsigned spins = 0; __builtin_amdgcn_readfirstlane(__hip_atomic_load(ctl + word, RLX_AGENT)) < want;) {
      __builtin_amdgcn_s_sleep(PSL);
      if (spin_fail(spins, ctl, dead)) break;
    }
  }
  asm volatile("s_barrier" ::: "memory");
}
__device__ __forceinline__ int wg_next(LAS unsigned char* L, gu32* ctl, int done, int queue) {
  LAS int* slot = (LAS int*)(L + G_STAGE + 64);
  asm volatile("s_waitcnt vmcnt(0) lgkmcnt(0)\n\ts_barrier" ::: "memory");
  if (threadIdx.x == 0) {
    if (done >= 0) __hip_atomic_fetch_add(ctl + done, 1u, RLX_AGENT);
    *slot = (int)__hip_atomic_fetch_add(ctl + queue, 1u, RLX_AGENT);
  }
  asm volatile("s_waitcnt vmcnt(0) lgkmcnt(0)\n\ts_barrier" ::: "memory");
  return __builtin_amdgcn_readfirstlane(*slot);
}
template <int N>
__device__ __forceinline__ v4u sum_bf16(const v4u (&v)[N]) {
  v4u o;
#pragma unroll
  for (int j = 0; j < 4; ++j) {
    float lo = 0.f, hi = 0.f;
#pragma unroll
    for (int k = 0; k < N; ++k) { lo += fbits(v[k][j] << 16); hi += fbits(v[k][j] & 0xFFFF0000u); }
    o[j] = cvt_pk(lo, hi);
  }
  return o;
}
#define OSTAMP(k)
__device__ __forceinline__ void outA_item(const Args& a, LAS unsigned char* L, int u, bool ready, unsigned& pol) {
  gu32* ctl = (gu32*)(a.base + CTL_OFF);
  const int t = u >> 2, kq = u & 3, win = (int)(SLAB_OFF >> 21) + (t >> 1);
  unsigned char* slab = a.base + SLAB_OFF + ((size_t)t << 20);
  if (!ready) wg_wait(ctl, CTL_OX + 64 * ((t >> 2) * 4 + kq), 32u);
  gemm_tiles<AUX_SC1>(L, 1, [&](int) {
    GItem it = g_plain((const u16*)(a.base + XQKV_OFF + ((size_t)kq << 23) + (t >> 2) * 8 * 16384), 128, far_off(pol, (int)(XQKV_OFF >> 21)),
                       a.Wo + (size_t)kq * 1024 * 1024 + (t & 3) * 256, 1024, (u16*)(slab + (kq << 18)), 256, 16);
    it.c16 = 8192; it.c32 = 16384; it.c128 = 65536; it.cwin = win;
    return it; }, ctl, pol);
  const bool share = t >= 16;
  const int arrived = wg_next(L, ctl, -1, CTL_O4 + 64 * t);
  if (arrived == 3 || share) {
    if (arrived != 3) wg_wait(ctl, CTL_O4 + 64 * t, 4u);
    __amdgpu_buffer_rsrc_t rs = mk_rsrc(slab + far_off(pol, win), (1 << 20) - 8192);
    for (int r = share ? 2 * kq : 0; r < (share ? 2 * kq + 2 : 8); ++r) {
      v4u v[4][4];
#pragma unroll
      for (int q = 0; q < 4; ++q)
#pragma unroll
        for (int k = 0; k < 4; ++k) v[q][k] = __builtin_amdgcn_raw_buffer_load_b128(rs, threadIdx.x * 16, (k << 18) + (r * 2 + (q >> 1)) * 16384 + (q & 1) * 4096, AUX_SC1);
#pragma unroll
      for (int q = 0; q < 4; ++q)
        *(GAS v4u*)(a.out + (size_t)((t >> 2) * 256 + (r * 4 + q) * 8 + (threadIdx.x >> 5)) * 2048 + (t & 3) * 256 + (threadIdx.x & 31) * 8) = sum_bf16<4>(v[q]);
    }
  }
}
__device__ __forceinline__ void outA_late(const Args& a, LAS unsigned char* L, int u, unsigned& pol) {
  gu32* ctl = (gu32*)(a.base + CTL_OFF);
  const int kq = u & 7, t = u >> 3, row0 = NRB1 * 256 + (t >> 3) * 128;
  unsigned char* slab = a.base + SLAB2_OFF + ((size_t)t << 19);
  const int win = (int)(SLAB2_OFF >> 21) + (t >> 2);
  wg_wait(ctl, CTL_OX + 64 * ((NRB1 + (t >> 3)) * 4 + (kq >> 1)), 32u);
  gemm_small<AUX_SC1>(L, 1, [&](int) {
    GItem it = g_plain((const u16*)(a.base + XQKV_OFF + ((size_t)kq << 22) + (row0 >> 5) * 16384), 128, far_off(pol, (int)(XQKV_OFF >> 21)),
                       a.Wo + (size_t)kq * 512 * 1024 + (t & 7) * 128, 1024, (u16*)(slab + (kq << 16)), 128, 8);
    it.c32 = 8192; it.cwin = win;
    it.flag = CTL_OT + 64 * t;
    return it; }, ctl, pol);
  wg_wait(ctl, CTL_OT + 64 * t, 8u);
  __amdgpu_buffer_rsrc_t rs = mk_rsrc(slab + far_off(pol, win), 8 * 65536 - 8192);
  v4u v[8];
#pragma unroll
  for (int k = 0; k < 8; ++k) v[k] = __builtin_amdgcn_raw_buffer_load_b128(rs, (kq >> 1) * 16384 + (kq & 1) * 4096 + threadIdx.x * 16, k * 65536, AUX_SC1);
  *(GAS v4u*)(a.out + (size_t)(row0 + kq * 16 + (threadIdx.x >> 4)) * 2048 + (t & 7) * 128 + (threadIdx.x & 15) * 8) = sum_bf16<8>(v);
}
__device__ __forceinline__ void outA_last(const Args& a, LAS unsigned char* L, int u, bool chain, bool& more, unsigned& pol) {
  gu32* ctl = (gu32*)(a.base + CTL_OFF);
  const int kq = u >> 3, t = u & 7, row0 = SEQ - 128 * NLAST;
  unsigned char* slab = a.base + SLAB2_OFF + ((size_t)(4 + t) << 20);
  const int win = (int)(SLAB2_OFF >> 21) + 2 + (t >> 1);
  GItem it = g_plain((const u16*)(a.base + XQKV_OFF + ((size_t)kq << 21) + (row0 >> 5) * 16384), 128, far_off(pol, (int)(XQKV_OFF >> 21) + kq),
                     a.Wo + (size_t)kq * 256 * 1024 + t * 128, 1024, (u16*)(slab + (kq << 16)), 128, 4);
  it.c32 = 8192; it.cwin = win;
  it.flag = CTL_OT + 64 * (8 + t);
  int n = 0;
  unsigned mine = 0;
  if (chain) {
    LAS unsigned* slot = (LAS unsigned*)(L + G_STAGE + 64);
    if (threadIdx.x < 64) {
      const int l = threadIdx.x;
      unsigned v = 0;
      if (l < 3) v = __hip_atomic_fetch_add(ctl + CTL_CLAIM + 4 * u + l, 1u, RLX_AGENT);
      else if (l == 3) v = __hip_atomic_load(ctl + CTL_LQ, RLX_AGENT) >= 64u;
      const unsigned z = (unsigned)__ballot(v == 0u) & 15u;
      if (l == 0) *slot = z;
    }
    asm volatile("s_waitcnt lgkmcnt(0)\n\ts_barrier" ::: "memory");
    mine = __builtin_amdgcn_readfirstlane(*slot);
    more = mine & 8u;
    if (!(mine & 7u)) return;
  }
  gemm_last<AUX_SC1>(L, it, 1 << 22, [&]() {
    for (; n < 3;) {
      const int j = n++;
      if (!chain && wg_next(L, ctl, -1, CTL_CLAIM + 4 * u + j) == 0) mine |= 1u << j;
      if (!((mine >> j) & 1)) continue;
      wg_wait(ctl, CTL_OX + 64 * ((10 - NLAST + j) * 4 + (kq >> 2)), 32u);
      return j;
    }
    return -1; }, ctl, pol);
  __amdgpu_buffer_rsrc_t rs = mk_rsrc(slab + far_off(pol, win), ((NLAST - 1) * 128 + 16) * 65536 - 8192);
  const int w = __builtin_amdgcn_readfirstlane(threadIdx.x >> 7), tl = threadIdx.x & 127;
  for (int j = 2 - w; j >= 0; j -= 2) {
    if (!((mine >> j) & 1)) continue;
    bool dead = false;
    for (unsigned spins = 0; __builtin_amdgcn_readfirstlane(__hip_atomic_load(ctl + CTL_OT + 64 * (8 + 8 * j + t), RLX_AGENT)) < 16u;)
      if (spin_fail(spins, ctl, dead)) break;
    __builtin_amdgcn_fence(__ATOMIC_ACQUIRE, "wavefront");
    LSTAMP(6);
    v4u v[16];
#pragma unroll
    for (int k = 0; k < 16; ++k) v[k] = __builtin_amdgcn_raw_buffer_load_b128(rs, (kq >> 2) * 16384 + (kq & 3) * 2048 + tl * 16, (j * 128 + k) * 65536, AUX_SC1);
    *(GAS v4u*)(a.out + (size_t)(row0 + j * 128 + kq * 8 + (tl >> 4)) * 2048 + t * 128 + (tl & 15) * 8) = sum_bf16<16>(v);
  }
}
__device__ __forceinline__ void proj_role(const Args& a, LAS unsigned char* L, int x, int i, unsigned& pol) {
  gu32* ctl = (gu32*)(a.base + CTL_OFF);
  gemm_small<AUX_SC1>(L, i < 16 ? 4 : i < 24 ? 2 : 0, [&](int k) { return small_item(a, x, i, k); }, ctl, pol);
  gemm_tiles<AUX_SC1>(L, (i >= 24 && i < 28) ? 4 : 3, [&](int k) { return proj_item(a, x, big_id(i, k)); }, ctl, pol);
  OSTAMP(1);
}
__device__ __forceinline__ void ticket_role(const Args& a, LAS unsigned char* L, int x, unsigned& pol) {
  gu32* ctl = (gu32*)(a.base + CTL_OFF);
  LAS int* slot = (LAS int*)(L + G_STAGE + 64);
  int done = -1;
  bool attn_first = false;
  unsigned empty = 0;
  for (;;) {
    unsigned v = 0;
    const int l = threadIdx.x;
    const bool draw = attn_first && !((empty >> x) & 1);
    if (l < 38) v = __hip_atomic_load(ctl + (l < 24 ? CTL_OX + 64 * l : l < 28 ? CTL_AQ + 64 * (l - 24) : l < 36 ? CTL_ATT + 64 * (l - 28) : l == 36 ? CTL_B : CTL_OQ), RLX_AGENT);
    if (l == 38 && draw) v = __hip_atomic_fetch_add(ctl + CTL_AQ + 64 * x, 1u, RLX_AGENT);
    asm volatile("s_waitcnt vmcnt(0) lgkmcnt(0)\n\ts_barrier" : "+v"(v)::"memory");
    if (l < 64) {
      if (done >= 0 && l == 0) __hip_atomic_fetch_add(ctl + done, 1u, RLX_AGENT);
      const unsigned long long ge32 = __ballot(v >= 32u);
      const int bok = __builtin_amdgcn_readlane(v, 36) >= 96u ? 4096 : 0, head = __builtin_amdgcn_readlane(v, 37);
      int item = -1;
      if (draw) {
        const int k = __builtin_amdgcn_readlane(v, 38);
        if (k < 64) item = (256 + x * 64 + k) | bok; else empty |= 1u << x;
      }
      empty |= (unsigned)(__ballot(v >= 64u) >> 24) & 15u;
      attn_first = head >= G4FIRST;
      auto draw_a = [&]() {
        const int u = __builtin_amdgcn_readfirstlane(l == 0 ? __hip_atomic_fetch_add(ctl + CTL_OQ, 1u, RLX_AGENT) : 0u);
        if (u < NRB1 * 16) item = u | (((ge32 >> ((u >> 4) * 4 + (u & 3))) & 1) ? 4096 : 0);
      };
      if (item < 0 && !attn_first && ((ge32 >> ((head >> 4) * 4)) & 15) == 15) draw_a();
      for (int q = 0; q < 4 && item < 0; ++q) {
        const int xq = (x + q) & 3;
        if (!((empty >> xq) & 1)) {
          const int k = __builtin_amdgcn_readfirstlane(l == 0 ? __hip_atomic_fetch_add(ctl + CTL_AQ + 64 * xq, 1u, RLX_AGENT) : 0u);
          if (k < 64) item = (256 + xq * 64 + k) | bok; else empty |= 1u << xq;
        }
      }
      if (item < 0 && head < NRB1 * 16) draw_a();
      if (item < 0) {
        const int k = __builtin_amdgcn_readfirstlane(l == 0 ? __hip_atomic_fetch_add(ctl + CTL_GQ, 1u, RLX_AGENT) : 0u);
        if (k < 128) {
          item = (128 + k) | (((ge32 >> (28 + (k >> 4))) & 1) ? 4096 : 0);
        } else {
          const int u = __builtin_amdgcn_readfirstlane(l == 0 ? __hip_atomic_fetch_add(ctl + CTL_LQ, 1u, RLX_AGENT) : 0u);
          item = u < 64 ? 512 + u : -2;
        }
      }
      if (l == 0) *slot = item;
    }
    done = -1;
    asm volatile("s_waitcnt lgkmcnt(0)\n\ts_barrier" ::: "memory");
    int item = __builtin_amdgcn_readfirstlane(*slot);
    if (item == -2) break;
    const bool ready = item & 4096;
    item &= 4095;
    asm volatile("" : "+s"(pol));
    if (item < 128) {
      outA_item(a, L, item, ready, pol);
    } else if (item < 256) {
      const int k = item - 128;
      if (!ready) wg_wait(ctl, CTL_ATT + 64 * (k >> 4), 32u);
      gemm_small<0>(L, 1, [&](int) {
        return g_plain((const u16*)(a.base + BO_OFF) + (size_t)(k >> 3) * 128 * 1024, 1024, -1, a.Wob + (k & 7) * 128, 1024,
                       a.out + (size_t)(k >> 3) * 128 * 2048 + 1024 + (k & 7) * 128, 2048, 16); }, ctl, pol);
    } else if (item >= 512) {
      outA_late(a, L, item - 512, pol);
    } else {
      const int xq = (item >> 6) & 3, k = item & 63;
      if (!ready) wg_wait(ctl, CTL_B, 96u);
      attn_item<AUX_SC1>(L, a.base + BQKV_OFF + far_off(pol, (int)(BQKV_OFF >> 21) + xq), (u16*)(a.base + BO_OFF), xq * 8 + (k & 7), k >> 3);
      done = CTL_ATT + 64 * (k >> 3);
      OSTAMP(2);
    }
  }
}
__global__ void __launch_bounds__(256, 1) k_main(Args a) {
  extern __shared__ __attribute__((aligned(16))) unsigned char lds[];
  LAS unsigned char* L = (LAS unsigned char*)lds;
  const int b = blockIdx.x, u = (b >> 3) * 4 + (b & 3);
  const bool chain = (b & 7) < 4;
  unsigned pol = 2u;
  OSTAMP(0);
  if (chain) {
    ChainArgs ca{a.Wq, a.Wk, a.Wv, a.Wo, a.base};
    chain_role(ca, L, u);
    pol = __builtin_amdgcn_readfirstlane(__hip_atomic_load((gu32*)(a.base + CTL_OFF) + CTL_POL, RLX_AGENT)) & 1u;
    OSTAMP(3);
  } else {
    proj_role(a, L, b & 3, b >> 3, pol);
  }
  bool more = true;
  for (int ph = 0; ph < 2; ++ph) {
    if ((ph == 0) == chain) {
      outA_last(a, L, u, chain, more, pol);
    } else if (more) {
      ticket_role(a, L, b & 3, pol);
      if (!chain) OSTAMP(3);
    }
  }
  OSTAMP(4);
}
}

extern "C" void kernel_launch(void* const* d_in, const int* in_sizes, int n_in, void* d_out, int out_size, void* d_ws, size_t ws_size,
                              hipStream_t stream) {
  unsigned char* base = (unsigned char*)(((uintptr_t)d_ws + (2u << 20) - 1) & ~(uintptr_t)((2u << 20) - 1));
  Args a{(const u16*)d_in[0], (const u16*)d_in[2], (const u16*)d_in[3], (const u16*)d_in[4], (const u16*)d_in[5],
         (const u16*)d_in[7], (const u16*)d_in[8], (const u16*)d_in[9], (u16*)d_out, base};
  (void)hipFuncSetAttribute((const void*)k_main, hipFuncAttributeMaxDynamicSharedMemorySize, GEMM_LDS);
  k_init<<<16, 256, 0, stream>>>(base, (const u16*)d_in[1]);
  k_main<<<256, 256, GEMM_LDS, stream>>>(a);
}
```
